```python
import torch

def _softmax(x, axis=-1):
    return torch.softmax(x, dim=axis)
def _gelu(x):
    return torch.nn.functional.gelu(x, approximate="tanh")
def _silu(x):
    return torch.nn.functional.silu(x)
def _sigmoid(x):
    return torch.sigmoid(x)
def _layernorm(x, axis=-1, eps=1e-5):
    m = x.mean(dim=axis, keepdim=True)
    v = x.var(dim=axis, keepdim=True, unbiased=False)
    return (x - m) / torch.sqrt(v + eps)
def _scatter_add(tgt, idx, src, axis=0):
    return tgt.clone().index_add_(axis, idx, src)
def _segment_sum(src, seg, n):
    out = torch.zeros((n,) + src.shape[1:], dtype=src.dtype)
    return out.index_add_(0, seg, src)
def _pad(x, pw):
    flat = tuple(v for pair in reversed(pw) for v in pair)
    return torch.nn.functional.pad(x, flat)

kernel_name = 'standalone__ssm_conv_s5__prompt'

def setup_inputs():

    _w = (lambda a: (lambda t: t.to(torch.bfloat16) if t.is_floating_point() else t)(torch.as_tensor(a)))
    B, S, D = 1, 2048, 1024
    P = D
    Lambda_re = -0.5 * (lambda _x: torch.exp(torch.as_tensor(_x)))((lambda *s: torch.randn(*s, dtype=torch.bfloat16) * 0.2)(P))
    Lambda_im = (lambda *s: torch.randn(*s, dtype=torch.bfloat16) * 0.4)(P) * 1.0
    B_re = (lambda *s: torch.randn(*s, dtype=torch.bfloat16) * 0.2)(P, D) * (D ** -0.5)
    B_im = (lambda *s: torch.randn(*s, dtype=torch.bfloat16) * 0.2)(P, D) * (D ** -0.5)
    C_re = (lambda *s: torch.randn(*s, dtype=torch.bfloat16) * 0.2)(D, P) * (P ** -0.5)
    C_im = (lambda *s: torch.randn(*s, dtype=torch.bfloat16) * 0.2)(D, P) * (P ** -0.5)
    log_dt = (lambda *s: torch.randn(*s, dtype=torch.bfloat16) * 0.2)(P) * 0.3 - 2.0
    Dskip = (lambda *s: torch.randn(*s, dtype=torch.bfloat16) * 0.05)(D)
    return {
        "x": _w((lambda *s: torch.randn(*s, dtype=torch.bfloat16) * 0.05)(B, S, D)),
        "Lambda_re": _w(Lambda_re),
        "Lambda_im": _w(Lambda_im),
        "B_re": _w(B_re),
        "B_im": _w(B_im),
        "C_re": _w(C_re),
        "C_im": _w(C_im),
        "log_dt": _w(log_dt),
        "Dskip": _w(Dskip),
    }

def reference(x, Lambda_re, Lambda_im, B_re, B_im, C_re, C_im, log_dt, Dskip):
    Bsz, L, H = x.shape
    Lam = Lambda_re + 1j * Lambda_im
    Bmat = B_re + 1j * B_im
    Cmat = C_re + 1j * C_im
    dt = torch.exp(log_dt)
    Lam_bar = torch.exp(dt * Lam)
    B_bar = ((Lam_bar - 1.0) / Lam)[:, None] * Bmat
    Bu = torch.einsum("bld,pd->blp", x + 0j, B_bar)
    h = torch.zeros_like(Bu[:, 0, :])
    hs = []
    for t in range(L):
        h = Lam_bar[None, :] * h + Bu[:, t, :]
        hs.append(h)
    hseq = torch.stack(hs, 1)
    y = 2.0 * torch.einsum("blp,dp->bld", hseq, Cmat).real + x * Dskip[None, None, :]
    return y
```

```cpp
#include <hip/hip_runtime.h>
#include <hip/hip_bf16.h>
#include <stdint.h>

typedef __attribute__((ext_vector_type(8))) short bf16x8;
typedef __attribute__((ext_vector_type(4))) float f32x4;
typedef __attribute__((ext_vector_type(4))) unsigned u32x4;
typedef __attribute__((ext_vector_type(2))) unsigned u32x2;
typedef unsigned short u16;
typedef unsigned long long u64;

#define GAS __attribute__((address_space(1)))
#define LAS __attribute__((address_space(3)))
typedef GAS u64 gu64;

constexpr int SEQ = 2048, DM = 1024, PS = 1024;

template <int N> __device__ __forceinline__ void wait_vm() { asm volatile("s_waitcnt vmcnt(%0)" ::"n"(N) : "memory"); }
__device__ __forceinline__ void bar_lgkm() { asm volatile("s_waitcnt lgkmcnt(0)\n\ts_barrier" ::: "memory"); }
__device__ __forceinline__ void bar_raw() { asm volatile("s_barrier" ::: "memory"); }

__device__ __forceinline__ float bf2f(u16 v) { return __uint_as_float(((unsigned)v) << 16); }
__device__ __forceinline__ unsigned pack_bf16(float lo, float hi) {
  __hip_bfloat16 a = __float2bfloat16(lo), b = __float2bfloat16(hi);
  return (unsigned)__bfloat16_as_ushort(a) | ((unsigned)__bfloat16_as_ushort(b) << 16);
}
__device__ __forceinline__ float rbf(float v) { return __bfloat162float(__float2bfloat16(v)); }

__device__ __forceinline__ void glds16(const void* g, void* l) {
  __builtin_amdgcn_global_load_lds((const GAS void*)g, (LAS void*)l, 16, 0, 0);
}

struct cpx { float r, i; };
__device__ __forceinline__ cpx cmul(cpx a, cpx b) { return {a.r * b.r - a.i * b.i, a.r * b.i + a.i * b.r}; }
__device__ __forceinline__ cpx cfma(cpx a, cpx b, cpx c) {
  return {fmaf(a.r, b.r, fmaf(-a.i, b.i, c.r)), fmaf(a.r, b.i, fmaf(a.i, b.r, c.i))};
}
__device__ __forceinline__ cpx csq(cpx a) { return cmul(a, a); }

__device__ __forceinline__ void disc(const u16* lre, const u16* lim, const u16* ldt, int p, cpx& lam, cpx& coef) {
  float lr = bf2f(lre[p]), li = bf2f(lim[p]);
  float dt = rbf(expf(bf2f(ldt[p])));
  float zr = dt * lr, zi = dt * li;
  float e = expf(zr), s, c;
  sincosf(zi, &s, &c);
  lam.r = e * c;
  lam.i = e * s;
  float nr = lam.r - 1.0f, ni = lam.i;
  float den = lr * lr + li * li;
  coef.r = (nr * lr + ni * li) / den;
  coef.i = (ni * lr - nr * li) / den;
}

constexpr int K1_NST = 4;
constexpr int K1_STAGE = 32768;
constexpr int K1_TAB = K1_NST * K1_STAGE;
constexpr int K1_LAM = K1_TAB;
constexpr int K1_SEG = K1_TAB + 512;
constexpr int K1_PART = K1_SEG + 4096;
constexpr int K1_LDS = K1_PART + 2048;
constexpr int K1_NKT = DM / 64;

__global__ __launch_bounds__(512, 2) void k1_in_scan(const u16* __restrict__ x, const u16* __restrict__ lre,
                                                     const u16* __restrict__ lim, const u16* __restrict__ bre,
                                                     const u16* __restrict__ bim, const u16* __restrict__ ldt,
                                                     u16* __restrict__ hbuf, u64* __restrict__ gran) {
  __shared__ __attribute__((aligned(1024))) char smem[K1_LDS];
  const int tid = threadIdx.x, lane = tid & 63;
  const int wave = __builtin_amdgcn_readfirstlane(tid >> 6);
  const int b = blockIdx.x, xcd = b & 7, idx = b >> 3;
  const int cm = (xcd >> 1) * 4 + (idx >> 3);
  const int cn = (xcd & 1) * 8 + (idx & 7);

  if (wave >= 4) {
    const int w = wave - 4;
    unsigned aoff[4], boff[4];
#pragma unroll
    for (int i = 0; i < 4; ++i) {
      int row = 8 * (4 * w + i) + (lane >> 3);
      int chs = (lane & 7) ^ ((row >> 1) & 7);
      int hm = row >> 6, s = (row >> 4) & 3, r = row & 15;
      int t = 64 * hm + 16 * (r >> 2) + 4 * s + (r & 3);
      aoff[i] = (unsigned)(t * DM + chs * 8) * 2u;
      int ch = 32 * hm + 2 * r + (s & 1);
      boff[i] = (unsigned)(ch * DM + chs * 8) * 2u;
    }
    const char* abase = (const char*)x + (size_t)cm * 128 * DM * 2;
    const char* bbase = (const char*)((w & 1) ? bim : bre) + (size_t)cn * 64 * DM * 2;
    auto issue = [&](int kt) {
      char* dst = smem + (kt % K1_NST) * K1_STAGE + w * 4096;
#pragma unroll
      for (int i = 0; i < 4; ++i) {
        glds16(abase + kt * 128 + aoff[i], dst + i * 1024);
        glds16(bbase + kt * 128 + boff[i], dst + 16384 + i * 1024);
      }
    };
#pragma unroll
    for (int kt = 0; kt < K1_NST; ++kt) issue(kt);
    wait_vm<8 * (K1_NST - 1)>();
    bar_raw();
#pragma unroll
    for (int kt = 0; kt < K1_NKT - 1; ++kt) {
      constexpr int dummy = 0;
      (void)dummy;
      int last = (kt + K1_NST - 1 < K1_NKT - 1) ? (kt + K1_NST - 1) : (K1_NKT - 1);
      int rem = last - (kt + 1);
      if (rem >= 3) wait_vm<24>();
      else if (rem == 2) wait_vm<16>();
      else if (rem == 1) wait_vm<8>();
      else wait_vm<0>();
      bar_raw();
      if (kt + K1_NST < K1_NKT) issue(kt + K1_NST);
    }
    cpx lam = *(const cpx*)(smem + K1_LAM + lane * 8);
    cpx l16 = csq(csq(csq(csq(lam))));
    cpx l128 = csq(csq(csq(l16)));
    cpx l512 = csq(csq(l128));
    bar_lgkm();
    gu64* gbase = (gu64*)gran + (size_t)cn * 16 * 128;
    if (w == 0 && cm < 15) {
      cpx a = {0.f, 0.f};
#pragma unroll
      for (int g = 0; g < 8; ++g) {
        cpx sgv = *(const cpx*)(smem + K1_SEG + (g * 64 + lane) * 8);
        a = cfma(l16, a, sgv);
      }
      gu64* gp = gbase + cm * 128 + lane * 2;
      __hip_atomic_store(gp, (1ull << 32) | (u64)__float_as_uint(a.r), __ATOMIC_RELAXED, __HIP_MEMORY_SCOPE_AGENT);
      __hip_atomic_store(gp + 1, (1ull << 32) | (u64)__float_as_uint(a.i), __ATOMIC_RELAXED, __HIP_MEMORY_SCOPE_AGENT);
    }
    cpx part = {0.f, 0.f};
    if (w < cm) {
      const int nj = (cm - w + 3) >> 2;
      u64 v[4][2];
      for (unsigned spins = 0;; ++spins) {
        bool ok = true;
#pragma unroll
        for (int i = 0; i < 4; ++i) {
          if (i < nj) {
            gu64* gp = gbase + (w + 4 * i) * 128 + lane * 2;
            v[i][0] = __hip_atomic_load(gp, __ATOMIC_RELAXED, __HIP_MEMORY_SCOPE_AGENT);
            v[i][1] = __hip_atomic_load(gp + 1, __ATOMIC_RELAXED, __HIP_MEMORY_SCOPE_AGENT);
            ok = ok && ((v[i][0] >> 32) == 1ull) && ((v[i][1] >> 32) == 1ull);
          }
        }
        if (__all(ok)) break;
        if (spins > (1u << 22)) break;
        __builtin_amdgcn_s_sleep(1);
      }
      cpx acc = {0.f, 0.f};
#pragma unroll
      for (int i = 0; i < 4; ++i) {
        if (i < nj) {
          cpx aj = {__uint_as_float((unsigned)v[i][0]), __uint_as_float((unsigned)v[i][1])};
          acc = cfma(l512, acc, aj);
        }
      }
      int e0 = cm - 1 - (w + 4 * (nj - 1));
      for (int e = 0; e < e0; ++e) acc = cmul(acc, l128);
      part = acc;
    }
    *(cpx*)(smem + K1_PART + (w * 64 + lane) * 8) = part;
    bar_lgkm();
    bar_lgkm();
  } else {
    const int wm = wave >> 1, wn = wave & 1;
    const int c = lane & 15, q = lane >> 4;
    cpx lamA, lamB, cfA, cfB;
    {
      int p0 = cn * 64 + 32 * wn + 2 * c;
      disc(lre, lim, ldt, p0, lamA, cfA);
      disc(lre, lim, ldt, p0 + 1, lamB, cfB);
      if (wm == 0 && q == 0) {
        f32x4 t4 = {lamA.r, lamA.i, lamB.r, lamB.i};
        *(f32x4*)(smem + K1_LAM + (32 * wn + 2 * c) * 8) = t4;
      }
    }
    const int f = (c >> 1) & 7;
    const unsigned a_rd0 = (unsigned)((64 * wm + c) * 128 + ((q ^ f) << 4));
    const unsigned b_rd0 = (unsigned)(16384 + (64 * wn + c) * 128 + ((q ^ f) << 4));
    f32x4 acc[4][4];
#pragma unroll
    for (int i = 0; i < 4; ++i)
#pragma unroll
      for (int j = 0; j < 4; ++j) acc[i][j] = (f32x4){0.f, 0.f, 0.f, 0.f};
    bf16x8 fa0[4], fb0[4], fa1[4], fb1[4];
    auto rd = [&](bf16x8 (&fa)[4], bf16x8 (&fb)[4], int kt, int ks) {
      const char* base = smem + (kt % K1_NST) * K1_STAGE;
      unsigned ao = a_rd0 ^ (ks << 6), bo = b_rd0 ^ (ks << 6);
#pragma unroll
      for (int i = 0; i < 4; ++i) {
        fa[i] = *(const bf16x8*)(base + ao + i * 2048);
        fb[i] = *(const bf16x8*)(base + bo + i * 2048);
      }
    };
    auto mm = [&](bf16x8 (&fa)[4], bf16x8 (&fb)[4]) {
#pragma unroll
      for (int i = 0; i < 4; ++i)
#pragma unroll
        for (int j = 0; j < 4; ++j)
          acc[i][j] = __builtin_amdgcn_mfma_f32_16x16x32_bf16(fa[i], fb[j], acc[i][j], 0, 0, 0);
    };
    bar_lgkm();
    rd(fa0, fb0, 0, 0);
#pragma unroll
    for (int kt = 0; kt < K1_NKT; ++kt) {
      rd(fa1, fb1, kt, 1);
      mm(fa0, fb0);
      if (kt < K1_NKT - 1) {
        bar_lgkm();
        rd(fa0, fb0, kt + 1, 0);
      }
      mm(fa1, fb1);
    }
    cpx aA = {0.f, 0.f}, aB = {0.f, 0.f};
#pragma unroll
    for (int s = 0; s < 4; ++s)
#pragma unroll
      for (int r = 0; r < 4; ++r) {
        aA = cfma(lamA, aA, cpx{acc[s][0][r], acc[s][2][r]});
        aB = cfma(lamB, aB, cpx{acc[s][1][r], acc[s][3][r]});
      }
    const int g = 4 * wm + q;
    {
      f32x4 t4 = {aA.r, aA.i, aB.r, aB.i};
      *(f32x4*)(smem + K1_SEG + (g * 64 + 32 * wn + 2 * c) * 8) = t4;
    }
    cpx l16A = csq(csq(csq(csq(lamA)))), l16B = csq(csq(csq(csq(lamB))));
    bar_lgkm();
    cpx wA = {0.f, 0.f}, wB = {0.f, 0.f}, pA = {1.f, 0.f}, pB = {1.f, 0.f};
#pragma unroll
    for (int gg = 0; gg < 7; ++gg) {
      f32x4 t4 = *(const f32x4*)(smem + K1_SEG + (gg * 64 + 32 * wn + 2 * c) * 8);
      if (gg < g) {
        wA = cfma(l16A, wA, cpx{t4[0], t4[1]});
        wB = cfma(l16B, wB, cpx{t4[2], t4[3]});
        pA = cmul(pA, l16A);
        pB = cmul(pB, l16B);
      }
    }
    bar_lgkm();
    {
      cpx cA = {0.f, 0.f}, cB = {0.f, 0.f};
#pragma unroll
      for (int i = 0; i < 4; ++i) {
        f32x4 t4 = *(const f32x4*)(smem + K1_PART + (i * 64 + 32 * wn + 2 * c) * 8);
        cA.r += t4[0]; cA.i += t4[1]; cB.r += t4[2]; cB.i += t4[3];
      }
      wA = cfma(pA, cA, wA);
      wB = cfma(pB, cB, wB);
    }
    cpx c2A = {2.f * cfA.r, 2.f * cfA.i}, c2B = {2.f * cfB.r, 2.f * cfB.i};
    char* obase = smem + (64 * wm + 16 * q) * 256 + wn * 64 + c * 4;
#pragma unroll
    for (int s = 0; s < 4; ++s)
#pragma unroll
      for (int r = 0; r < 4; ++r) {
        wA = cfma(lamA, wA, cpx{acc[s][0][r], acc[s][2][r]});
        wB = cfma(lamB, wB, cpx{acc[s][1][r], acc[s][3][r]});
        cpx hA = cmul(c2A, wA), hB = cmul(c2B, wB);
        *(unsigned*)(obase + (4 * s + r) * 256) = pack_bf16(hA.r, hB.r);
        *(unsigned*)(obase + (4 * s + r) * 256 + 128) = pack_bf16(-hA.i, -hB.i);
      }
    bar_lgkm();
  }
  {
    char* gdst = (char*)hbuf + (size_t)cm * 128 * 4096 + cn * 256;
#pragma unroll
    for (int i = 0; i < 4; ++i) {
      int e = tid + i * 512;
      int row = e >> 4, chk = e & 15;
      u32x4 v = *(const u32x4*)(smem + row * 256 + chk * 16);
      *(u32x4*)(gdst + (size_t)row * 4096 + chk * 16) = v;
    }
  }
}

constexpr int K2_NST = 6;
constexpr int K2_STAGE = 24576;
constexpr int K2_LDS = K2_NST * K2_STAGE;
constexpr int K2_NKT = 32;

__global__ __launch_bounds__(512, 2) void k2_out(const u16* __restrict__ hbuf, const u16* __restrict__ cre,
                                                 const u16* __restrict__ cim, const u16* __restrict__ x,
                                                 const u16* __restrict__ dsk, float* __restrict__ y) {
  __shared__ __attribute__((aligned(1024))) char smem[K2_LDS];
  const int tid = threadIdx.x, lane = tid & 63;
  const int wave = __builtin_amdgcn_readfirstlane(tid >> 6);
  const int b = blockIdx.x, xcd = b & 7, idx = b >> 3;
  const int tm = (xcd >> 1) * 4 + (idx >> 3);
  const int tn = (xcd & 1) * 8 + (idx & 7);

  if (wave >= 4) {
    const int w = wave - 4;
    unsigned hoff[4], coff[2];
#pragma unroll
    for (int i = 0; i < 4; ++i) {
      int row = 8 * (4 * w + i) + (lane >> 3);
      int chs = (lane & 7) ^ ((row >> 1) & 7);
      hoff[i] = (unsigned)(row * 4096 + chs * 16);
    }
#pragma unroll
    for (int i = 0; i < 2; ++i) {
      int row = 8 * (2 * w + i) + (lane >> 3);
      int chs = (lane & 7) ^ ((row >> 1) & 7);
      coff[i] = (unsigned)(row * 2048 + chs * 16);
    }
    const char* hb = (const char*)hbuf + (size_t)tm * 128 * 4096;
    const char* cb0 = (const char*)cre + (size_t)tn * 64 * 2048;
    const char* cb1 = (const char*)cim + (size_t)tn * 64 * 2048;
    auto issue = [&](int kt) {
      char* dst = smem + (kt % K2_NST) * K2_STAGE;
      const char* cb = ((kt & 1) ? cb1 : cb0) + (kt >> 1) * 128;
#pragma unroll
      for (int i = 0; i < 4; ++i) glds16(hb + kt * 128 + hoff[i], dst + (4 * w + i) * 1024);
#pragma unroll
      for (int i = 0; i < 2; ++i) glds16(cb + coff[i], dst + 16384 + (2 * w + i) * 1024);
    };
#pragma unroll
    for (int kt = 0; kt < K2_NST; ++kt) issue(kt);
    wait_vm<6 * (K2_NST - 1)>();
    bar_raw();
#pragma unroll
    for (int kt = 0; kt < K2_NKT - 1; ++kt) {
      int last = (kt + K2_NST - 1 < K2_NKT - 1) ? (kt + K2_NST - 1) : (K2_NKT - 1);
      int rem = last - (kt + 1);
      if (rem >= 4) wait_vm<24>();
      else if (rem == 3) wait_vm<18>();
      else if (rem == 2) wait_vm<12>();
      else if (rem == 1) wait_vm<6>();
      else wait_vm<0>();
      bar_raw();
      if (kt + K2_NST < K2_NKT) issue(kt + K2_NST);
    }
  } else {
    const int wt = wave >> 1, wd = wave & 1;
    const int c = lane & 15, q = lane >> 4;
    f32x4 acc[2][4];
#pragma unroll
    for (int s = 0; s < 2; ++s) {
      int d0 = tn * 64 + 32 * wd + 16 * s + 4 * q;
      u32x2 dv = *(const u32x2*)(dsk + d0);
      float d0f = __uint_as_float(dv[0] << 16), d1f = __uint_as_float(dv[0] & 0xffff0000u);
      float d2f = __uint_as_float(dv[1] << 16), d3f = __uint_as_float(dv[1] & 0xffff0000u);
#pragma unroll
      for (int j = 0; j < 4; ++j) {
        int t = tm * 128 + 64 * wt + 16 * j + c;
        u32x2 xv = *(const u32x2*)(x + (size_t)t * DM + d0);
        acc[s][j][0] = rbf(__uint_as_float(xv[0] << 16) * d0f);
        acc[s][j][1] = rbf(__uint_as_float(xv[0] & 0xffff0000u) * d1f);
        acc[s][j][2] = rbf(__uint_as_float(xv[1] << 16) * d2f);
        acc[s][j][3] = rbf(__uint_as_float(xv[1] & 0xffff0000u) * d3f);
      }
    }
    const int f = (c >> 1) & 7;
    const unsigned h_rd0 = (unsigned)((64 * wt + c) * 128 + ((q ^ f) << 4));
    const unsigned c_rd0 = (unsigned)(16384 + (32 * wd + c) * 128 + ((q ^ f) << 4));
    bf16x8 fh0[4], fc0[2], fh1[4], fc1[2];
    auto rd = [&](bf16x8 (&fh)[4], bf16x8 (&fc)[2], int kt, int ks) {
      const char* base = smem + (kt % K2_NST) * K2_STAGE;
      unsigned ho = h_rd0 ^ (ks << 6), co = c_rd0 ^ (ks << 6);
#pragma unroll
      for (int i = 0; i < 2; ++i) fc[i] = *(const bf16x8*)(base + co + i * 2048);
#pragma unroll
      for (int i = 0; i < 4; ++i) fh[i] = *(const bf16x8*)(base + ho + i * 2048);
    };
    auto mm = [&](bf16x8 (&fh)[4], bf16x8 (&fc)[2]) {
#pragma unroll
      for (int s = 0; s < 2; ++s)
#pragma unroll
        for (int j = 0; j < 4; ++j)
          acc[s][j] = __builtin_amdgcn_mfma_f32_16x16x32_bf16(fc[s], fh[j], acc[s][j], 0, 0, 0);
    };
    bar_lgkm();
    rd(fh0, fc0, 0, 0);
#pragma unroll
    for (int kt = 0; kt < K2_NKT; ++kt) {
      rd(fh1, fc1, kt, 1);
      mm(fh0, fc0);
      if (kt < K2_NKT - 1) {
        bar_lgkm();
        rd(fh0, fc0, kt + 1, 0);
      }
      mm(fh1, fc1);
    }
#pragma unroll
    for (int s = 0; s < 2; ++s)
#pragma unroll
      for (int j = 0; j < 4; ++j) {
        int t = tm * 128 + 64 * wt + 16 * j + c;
        int d0 = tn * 64 + 32 * wd + 16 * s + 4 * q;
        *(f32x4*)(y + (size_t)t * DM + d0) = acc[s][j];
      }
  }
}

extern "C" void kernel_launch(void* const* d_in, const int* in_sizes, int n_in, void* d_out, int out_size,
                              void* d_ws, size_t ws_size, hipStream_t stream) {
  const u16* x = (const u16*)d_in[0];
  const u16* lre = (const u16*)d_in[1];
  const u16* lim = (const u16*)d_in[2];
  const u16* bre = (const u16*)d_in[3];
  const u16* bim = (const u16*)d_in[4];
  const u16* cre = (const u16*)d_in[5];
  const u16* cim = (const u16*)d_in[6];
  const u16* ldt = (const u16*)d_in[7];
  const u16* dsk = (const u16*)d_in[8];
  float* y = (float*)d_out;
  u64* gran = (u64*)d_ws;
  u16* hbuf = (u16*)((char*)d_ws + (1u << 20));
  hipMemsetAsync(gran, 0, 16 * 16 * 128 * 8, stream);
  k1_in_scan<<<256, 512, 0, stream>>>(x, lre, lim, bre, bim, ldt, hbuf, gran);
  k2_out<<<256, 512, 0, stream>>>(hbuf, cre, cim, x, dsk, y);
}
```

```cpp
#define EXP_SCHED
#include <hip/hip_runtime.h>
#include <hip/hip_bf16.h>
#include <stdint.h>

typedef __attribute__((ext_vector_type(8))) short bf16x8;
typedef __attribute__((ext_vector_type(4))) float f32x4;
typedef __attribute__((ext_vector_type(4))) unsigned u32x4;
typedef __attribute__((ext_vector_type(2))) unsigned u32x2;
typedef unsigned short u16;
typedef unsigned long long u64;

#define GAS __attribute__((address_space(1)))
#define LAS __attribute__((address_space(3)))
typedef GAS u64 gu64;

constexpr int SEQ = 2048, DM = 1024, PS = 1024;

template <int N> __device__ __forceinline__ void wait_vm() { asm volatile("s_waitcnt vmcnt(%0)" ::"n"(N) : "memory"); }
__device__ __forceinline__ void bar_lgkm() { asm volatile("s_waitcnt lgkmcnt(0)\n\ts_barrier" ::: "memory"); }
__device__ __forceinline__ void bar_raw() { asm volatile("s_barrier" ::: "memory"); }

__device__ __forceinline__ float bf2f(u16 v) { return __uint_as_float(((unsigned)v) << 16); }
__device__ __forceinline__ unsigned pack_bf16(float lo, float hi) {
  __hip_bfloat16 a = __float2bfloat16(lo), b = __float2bfloat16(hi);
  return (unsigned)__bfloat16_as_ushort(a) | ((unsigned)__bfloat16_as_ushort(b) << 16);
}
__device__ __forceinline__ float rbf(float v) { return __bfloat162float(__float2bfloat16(v)); }

__device__ __forceinline__ void glds16(const void* g, void* l) {
  __builtin_amdgcn_global_load_lds((const GAS void*)g, (LAS void*)l, 16, 0, 0);
}

#define TS(kern, slot) do {} while (0)
#define TSARG
struct cpx { float r, i; };
__device__ __forceinline__ cpx cmul(cpx a, cpx b) { return {a.r * b.r - a.i * b.i, a.r * b.i + a.i * b.r}; }
__device__ __forceinline__ cpx cfma(cpx a, cpx b, cpx c) {
  return {fmaf(a.r, b.r, fmaf(-a.i, b.i, c.r)), fmaf(a.r, b.i, fmaf(a.i, b.r, c.i))};
}
__device__ __forceinline__ cpx csq(cpx a) { return cmul(a, a); }

__device__ __forceinline__ void disc(const u16* lre, const u16* lim, const u16* ldt, int p, cpx& lam, cpx& coef) {
  float lr = bf2f(lre[p]), li = bf2f(lim[p]);
  float dt = rbf(expf(bf2f(ldt[p])));
  float zr = dt * lr, zi = dt * li;
  float e = expf(zr), s, c;
  sincosf(zi, &s, &c);
  lam.r = e * c;
  lam.i = e * s;
  float nr = lam.r - 1.0f, ni = lam.i;
  float den = lr * lr + li * li;
  coef.r = (nr * lr + ni * li) / den;
  coef.i = (ni * lr - nr * li) / den;
}

constexpr int K1_NST = 4;
constexpr int K1_STAGE = 32768;
constexpr int K1_TAB = K1_NST * K1_STAGE;
constexpr int K1_LAM = K1_TAB;
constexpr int K1_SEG = K1_TAB + 512;
constexpr int K1_PART = K1_SEG + 4096;
constexpr int K1_LDS = K1_PART + 2048;
constexpr int K1_NKT = DM / 64;

__global__ __launch_bounds__(512, 2) void k1_in_scan(const u16* __restrict__ x, const u16* __restrict__ lre,
                                                     const u16* __restrict__ lim, const u16* __restrict__ bre,
                                                     const u16* __restrict__ bim, const u16* __restrict__ ldt,
                                                     u16* __restrict__ hbuf, u64* __restrict__ gran TSARG) {
  __shared__ __attribute__((aligned(1024))) char smem[K1_LDS];
  const int tid = threadIdx.x, lane = tid & 63;
  const int wave = __builtin_amdgcn_readfirstlane(tid >> 6);
  const int b = blockIdx.x, xcd = b & 7, idx = b >> 3;
  const int cm = (xcd >> 1) * 4 + (idx >> 3);
  const int cn = (xcd & 1) * 8 + (idx & 7);
  if (wave == 0) TS(0, 0);
  if (wave == 4) TS(0, 16);

  if (wave >= 4) {
    const int w = wave - 4;
    unsigned aoff[4], boff[4];
#pragma unroll
    for (int i = 0; i < 4; ++i) {
      int row = 8 * (4 * w + i) + (lane >> 3);
      int chs = (lane & 7) ^ ((row >> 1) & 7);
      int hm = row >> 6, s = (row >> 4) & 3, r = row & 15;
      int t = 64 * hm + 16 * (r >> 2) + 4 * s + (r & 3);
      aoff[i] = (unsigned)(t * DM + chs * 8) * 2u;
      int ch = 32 * hm + 2 * r + (s & 1);
      boff[i] = (unsigned)(ch * DM + chs * 8) * 2u;
    }
    const char* abase = (const char*)x + (size_t)cm * 128 * DM * 2;
    const char* bbase = (const char*)((w & 1) ? bim : bre) + (size_t)cn * 64 * DM * 2;
    auto issue = [&](int kt) {
      char* dst = smem + (kt % K1_NST) * K1_STAGE + w * 4096;
      const int ktl = kt;
#pragma unroll
      for (int i = 0; i < 4; ++i) {
        glds16(abase + ktl * 128 + aoff[i], dst + i * 1024);
        glds16(bbase + ktl * 128 + boff[i], dst + 16384 + i * 1024);
      }
    };
#pragma unroll
    for (int kt = 0; kt < K1_NST; ++kt) issue(kt);
    wait_vm<8 * (K1_NST - 1)>();
    bar_raw();
#pragma unroll
    for (int kt = 0; kt < K1_NKT - 1; ++kt) {
      constexpr int dummy = 0;
      (void)dummy;
      int last = (kt + K1_NST - 1 < K1_NKT - 1) ? (kt + K1_NST - 1) : (K1_NKT - 1);
      int rem = last - (kt + 1);
      if (rem >= 3) wait_vm<24>();
      else if (rem == 2) wait_vm<16>();
      else if (rem == 1) wait_vm<8>();
      else wait_vm<0>();
      bar_raw();
      if (kt + K1_NST < K1_NKT) issue(kt + K1_NST);
    }
    cpx lam = *(const cpx*)(smem + K1_LAM + lane * 8);
    cpx l16 = csq(csq(csq(csq(lam))));
    cpx l128 = csq(csq(csq(l16)));
    cpx l512 = csq(csq(l128));
    bar_lgkm();
    gu64* gbase = (gu64*)gran + (size_t)cn * 16 * 128;
    if (w == 0 && cm < 15) {
      cpx a = {0.f, 0.f};
#pragma unroll
      for (int g = 0; g < 8; ++g) {
        cpx sgv = *(const cpx*)(smem + K1_SEG + (g * 64 + lane) * 8);
        a = cfma(l16, a, sgv);
      }
      gu64* gp = gbase + cm * 128 + lane * 2;
      __hip_atomic_store(gp, (1ull << 32) | (u64)__float_as_uint(a.r), __ATOMIC_RELAXED, __HIP_MEMORY_SCOPE_AGENT);
      __hip_atomic_store(gp + 1, (1ull << 32) | (u64)__float_as_uint(a.i), __ATOMIC_RELAXED, __HIP_MEMORY_SCOPE_AGENT);
    }
    cpx part = {0.f, 0.f};
    if (w < cm) {
      const int nj = (cm - w + 3) >> 2;
      u64 v[4][2];
      for (unsigned spins = 0;; ++spins) {
        bool ok = true;
#pragma unroll
        for (int i = 0; i < 4; ++i) {
          if (i < nj) {
            gu64* gp = gbase + (w + 4 * i) * 128 + lane * 2;
            v[i][0] = __hip_atomic_load(gp, __ATOMIC_RELAXED, __HIP_MEMORY_SCOPE_AGENT);
            v[i][1] = __hip_atomic_load(gp + 1, __ATOMIC_RELAXED, __HIP_MEMORY_SCOPE_AGENT);
            ok = ok && ((v[i][0] >> 32) == 1ull) && ((v[i][1] >> 32) == 1ull);
          }
        }
        if (__all(ok)) break;
        if (spins > (1u << 22)) break;
        __builtin_amdgcn_s_sleep(1);
      }
      cpx acc = {0.f, 0.f};
#pragma unroll
      for (int i = 0; i < 4; ++i) {
        if (i < nj) {
          cpx aj = {__uint_as_float((unsigned)v[i][0]), __uint_as_float((unsigned)v[i][1])};
          acc = cfma(l512, acc, aj);
        }
      }
      int e0 = cm - 1 - (w + 4 * (nj - 1));
      for (int e = 0; e < e0; ++e) acc = cmul(acc, l128);
      part = acc;
    }
    *(cpx*)(smem + K1_PART + (w * 64 + lane) * 8) = part;
    bar_lgkm();
    bar_lgkm();
  } else {
    const int wm = wave >> 1, wn = wave & 1;
    const int c = lane & 15, q = lane >> 4;
    cpx lamA, lamB, cfA, cfB;
    {
      int p0 = cn * 64 + 32 * wn + 2 * c;
      disc(lre, lim, ldt, p0, lamA, cfA);
      disc(lre, lim, ldt, p0 + 1, lamB, cfB);
      if (wm == 0 && q == 0) {
        f32x4 t4 = {lamA.r, lamA.i, lamB.r, lamB.i};
        *(f32x4*)(smem + K1_LAM + (32 * wn + 2 * c) * 8) = t4;
      }
    }
    const int f = (c >> 1) & 7;
    const unsigned a_rd0 = (unsigned)((64 * wm + c) * 128 + ((q ^ f) << 4));
    const unsigned b_rd0 = (unsigned)(16384 + (64 * wn + c) * 128 + ((q ^ f) << 4));
    f32x4 acc[4][4];
#pragma unroll
    for (int i = 0; i < 4; ++i)
#pragma unroll
      for (int j = 0; j < 4; ++j) acc[i][j] = (f32x4){0.f, 0.f, 0.f, 0.f};
    bf16x8 fa0[4], fb0[4], fa1[4], fb1[4];
    auto rd = [&](bf16x8 (&fa)[4], bf16x8 (&fb)[4], int kt, int ks) {
      const char* base = smem + (kt % K1_NST) * K1_STAGE;
      unsigned ao = a_rd0 ^ (ks << 6), bo = b_rd0 ^ (ks << 6);
#pragma unroll
      for (int i = 0; i < 4; ++i) {
        fa[i] = *(const bf16x8*)(base + ao + i * 2048);
        fb[i] = *(const bf16x8*)(base + bo + i * 2048);
      }
    };
    auto mm = [&](bf16x8 (&fa)[4], bf16x8 (&fb)[4]) {
#pragma unroll
      for (int i = 0; i < 4; ++i)
#pragma unroll
        for (int j = 0; j < 4; ++j)
          acc[i][j] = __builtin_amdgcn_mfma_f32_16x16x32_bf16(fa[i], fb[j], acc[i][j], 0, 0, 0);
    };
    if (wave == 0) TS(0, 1);
    bar_lgkm();
    if (wave == 0) TS(0, 2);
    rd(fa0, fb0, 0, 0);
#pragma unroll
    for (int kt = 0; kt < K1_NKT; ++kt) {
      rd(fa1, fb1, kt, 1);
      mm(fa0, fb0);
#pragma unroll
      for (int i = 0; i < 8; ++i) { __builtin_amdgcn_sched_group_barrier(0x100, 1, 0); __builtin_amdgcn_sched_group_barrier(0x8, 1, 0); }
      __builtin_amdgcn_sched_group_barrier(0x8, 8, 0);
      __builtin_amdgcn_sched_barrier(0);
      if (kt < K1_NKT - 1) {
        bar_lgkm();
        rd(fa0, fb0, kt + 1, 0);
      }
      mm(fa1, fb1);
      if (kt < K1_NKT - 1) {
#pragma unroll
      for (int i = 0; i < 8; ++i) { __builtin_amdgcn_sched_group_barrier(0x100, 1, 0); __builtin_amdgcn_sched_group_barrier(0x8, 1, 0); }
      __builtin_amdgcn_sched_group_barrier(0x8, 8, 0);
      }
      __builtin_amdgcn_sched_barrier(0);
    }
    if (wave == 0) TS(0, 3);
    cpx aA = {0.f, 0.f}, aB = {0.f, 0.f};
#pragma unroll
    for (int s = 0; s < 4; ++s)
#pragma unroll
      for (int r = 0; r < 4; ++r) {
        aA = cfma(lamA, aA, cpx{acc[s][0][r], acc[s][2][r]});
        aB = cfma(lamB, aB, cpx{acc[s][1][r], acc[s][3][r]});
      }
    const int g = 4 * wm + q;
    {
      f32x4 t4 = {aA.r, aA.i, aB.r, aB.i};
      *(f32x4*)(smem + K1_SEG + (g * 64 + 32 * wn + 2 * c) * 8) = t4;
    }
    cpx l16A = csq(csq(csq(csq(lamA)))), l16B = csq(csq(csq(csq(lamB))));
    bar_lgkm();
    if (wave == 0) TS(0, 4);
    cpx wA = {0.f, 0.f}, wB = {0.f, 0.f}, pA = {1.f, 0.f}, pB = {1.f, 0.f};
#pragma unroll
    for (int gg = 0; gg < 7; ++gg) {
      f32x4 t4 = *(const f32x4*)(smem + K1_SEG + (gg * 64 + 32 * wn + 2 * c) * 8);
      if (gg < g) {
        wA = cfma(l16A, wA, cpx{t4[0], t4[1]});
        wB = cfma(l16B, wB, cpx{t4[2], t4[3]});
        pA = cmul(pA, l16A);
        pB = cmul(pB, l16B);
      }
    }
    bar_lgkm();
    if (wave == 0) TS(0, 5);
    {
      cpx cA = {0.f, 0.f}, cB = {0.f, 0.f};
#pragma unroll
      for (int i = 0; i < 4; ++i) {
        f32x4 t4 = *(const f32x4*)(smem + K1_PART + (i * 64 + 32 * wn + 2 * c) * 8);
        cA.r += t4[0]; cA.i += t4[1]; cB.r += t4[2]; cB.i += t4[3];
      }
      wA = cfma(pA, cA, wA);
      wB = cfma(pB, cB, wB);
    }
    cpx c2A = {2.f * cfA.r, 2.f * cfA.i}, c2B = {2.f * cfB.r, 2.f * cfB.i};
    char* obase = smem + (64 * wm + 16 * q) * 256 + wn * 64 + c * 4;
#pragma unroll
    for (int s = 0; s < 4; ++s)
#pragma unroll
      for (int r = 0; r < 4; ++r) {
        wA = cfma(lamA, wA, cpx{acc[s][0][r], acc[s][2][r]});
        wB = cfma(lamB, wB, cpx{acc[s][1][r], acc[s][3][r]});
        cpx hA = cmul(c2A, wA), hB = cmul(c2B, wB);
        *(unsigned*)(obase + (4 * s + r) * 256) = pack_bf16(hA.r, hB.r);
        *(unsigned*)(obase + (4 * s + r) * 256 + 128) = pack_bf16(-hA.i, -hB.i);
      }
    bar_lgkm();
    if (wave == 0) TS(0, 6);
  }
  {
    char* gdst = (char*)hbuf + (size_t)cm * 128 * 4096 + cn * 256;
#pragma unroll
    for (int i = 0; i < 4; ++i) {
      int e = tid + i * 512;
      int row = e >> 4, chk = e & 15;
      u32x4 v = *(const u32x4*)(smem + row * 256 + chk * 16);
      *(u32x4*)(gdst + (size_t)row * 4096 + chk * 16) = v;
    }
  }
}

constexpr int K2_NST = 6;
constexpr int K2_STAGE = 24576;
constexpr int K2_LDS = K2_NST * K2_STAGE;
constexpr int K2_NKT = 32;

__global__ __launch_bounds__(512, 2) void k2_out(const u16* __restrict__ hbuf, const u16* __restrict__ cre,
                                                 const u16* __restrict__ cim, const u16* __restrict__ x,
                                                 const u16* __restrict__ dsk, float* __restrict__ y TSARG) {
  __shared__ __attribute__((aligned(1024))) char smem[K2_LDS];
  const int tid = threadIdx.x, lane = tid & 63;
  const int wave = __builtin_amdgcn_readfirstlane(tid >> 6);
  const int b = blockIdx.x, xcd = b & 7, idx = b >> 3;
  const int tm = (xcd >> 1) * 4 + (idx >> 3);
  const int tn = (xcd & 1) * 8 + (idx & 7);
  if (wave == 0) TS(1, 0);

  if (wave >= 4) {
    const int w = wave - 4;
    unsigned hoff[4], coff[2];
#pragma unroll
    for (int i = 0; i < 4; ++i) {
      int row = 8 * (4 * w + i) + (lane >> 3);
      int chs = (lane & 7) ^ ((row >> 1) & 7);
      hoff[i] = (unsigned)(row * 4096 + chs * 16);
    }
#pragma unroll
    for (int i = 0; i < 2; ++i) {
      int row = 8 * (2 * w + i) + (lane >> 3);
      int chs = (lane & 7) ^ ((row >> 1) & 7);
      coff[i] = (unsigned)(row * 2048 + chs * 16);
    }
    const char* hb = (const char*)hbuf + (size_t)tm * 128 * 4096;
    const char* cb0 = (const char*)cre + (size_t)tn * 64 * 2048;
    const char* cb1 = (const char*)cim + (size_t)tn * 64 * 2048;
    auto issue = [&](int kt) {
      char* dst = smem + (kt % K2_NST) * K2_STAGE;
      const int ktl = kt;
      const char* cb = ((ktl & 1) ? cb1 : cb0) + (ktl >> 1) * 128;
#pragma unroll
      for (int i = 0; i < 4; ++i) glds16(hb + ktl * 128 + hoff[i], dst + (4 * w + i) * 1024);
#pragma unroll
      for (int i = 0; i < 2; ++i) glds16(cb + coff[i], dst + 16384 + (2 * w + i) * 1024);
    };
#pragma unroll
    for (int kt = 0; kt < K2_NST; ++kt) issue(kt);
    wait_vm<6 * (K2_NST - 1)>();
    bar_raw();
#pragma unroll
    for (int kt = 0; kt < K2_NKT - 1; ++kt) {
      int last = (kt + K2_NST - 1 < K2_NKT - 1) ? (kt + K2_NST - 1) : (K2_NKT - 1);
      int rem = last - (kt + 1);
      if (rem >= 4) wait_vm<24>();
      else if (rem == 3) wait_vm<18>();
      else if (rem == 2) wait_vm<12>();
      else if (rem == 1) wait_vm<6>();
      else wait_vm<0>();
      bar_raw();
      if (kt + K2_NST < K2_NKT) issue(kt + K2_NST);
    }
  } else {
    const int wt = wave >> 1, wd = wave & 1;
    const int c = lane & 15, q = lane >> 4;
    f32x4 acc[2][4];
#pragma unroll
    for (int s = 0; s < 2; ++s) {
      int d0 = tn * 64 + 32 * wd + 16 * s + 4 * q;
      u32x2 dv = *(const u32x2*)(dsk + d0);
      float d0f = __uint_as_float(dv[0] << 16), d1f = __uint_as_float(dv[0] & 0xffff0000u);
      float d2f = __uint_as_float(dv[1] << 16), d3f = __uint_as_float(dv[1] & 0xffff0000u);
#pragma unroll
      for (int j = 0; j < 4; ++j) {
        int t = tm * 128 + 64 * wt + 16 * j + c;
        u32x2 xv = *(const u32x2*)(x + (size_t)t * DM + d0);
        acc[s][j][0] = rbf(__uint_as_float(xv[0] << 16) * d0f);
        acc[s][j][1] = rbf(__uint_as_float(xv[0] & 0xffff0000u) * d1f);
        acc[s][j][2] = rbf(__uint_as_float(xv[1] << 16) * d2f);
        acc[s][j][3] = rbf(__uint_as_float(xv[1] & 0xffff0000u) * d3f);
      }
    }
    const int f = (c >> 1) & 7;
    const unsigned h_rd0 = (unsigned)((64 * wt + c) * 128 + ((q ^ f) << 4));
    const unsigned c_rd0 = (unsigned)(16384 + (32 * wd + c) * 128 + ((q ^ f) << 4));
    bf16x8 fh0[4], fc0[2], fh1[4], fc1[2];
    auto rd = [&](bf16x8 (&fh)[4], bf16x8 (&fc)[2], int kt, int ks) {
      const char* base = smem + (kt % K2_NST) * K2_STAGE;
      unsigned ho = h_rd0 ^ (ks << 6), co = c_rd0 ^ (ks << 6);
#pragma unroll
      for (int i = 0; i < 2; ++i) fc[i] = *(const bf16x8*)(base + co + i * 2048);
#pragma unroll
      for (int i = 0; i < 4; ++i) fh[i] = *(const bf16x8*)(base + ho + i * 2048);
    };
    auto mm = [&](bf16x8 (&fh)[4], bf16x8 (&fc)[2]) {
#pragma unroll
      for (int s = 0; s < 2; ++s)
#pragma unroll
        for (int j = 0; j < 4; ++j)
          acc[s][j] = __builtin_amdgcn_mfma_f32_16x16x32_bf16(fc[s], fh[j], acc[s][j], 0, 0, 0);
    };
    if (wave == 0) TS(1, 1);
    bar_lgkm();
    if (wave == 0) TS(1, 2);
    rd(fh0, fc0, 0, 0);
#pragma unroll
    for (int kt = 0; kt < K2_NKT; ++kt) {
      rd(fh1, fc1, kt, 1);
      mm(fh0, fc0);
#pragma unroll
      for (int i = 0; i < 6; ++i) { __builtin_amdgcn_sched_group_barrier(0x100, 1, 0); __builtin_amdgcn_sched_group_barrier(0x8, 1, 0); }
      __builtin_amdgcn_sched_group_barrier(0x8, 2, 0);
      __builtin_amdgcn_sched_barrier(0);
      if (kt < K2_NKT - 1) {
        bar_lgkm();
        rd(fh0, fc0, kt + 1, 0);
      }
      mm(fh1, fc1);
      if (kt < K2_NKT - 1) {
#pragma unroll
      for (int i = 0; i < 6; ++i) { __builtin_amdgcn_sched_group_barrier(0x100, 1, 0); __builtin_amdgcn_sched_group_barrier(0x8, 1, 0); }
      __builtin_amdgcn_sched_group_barrier(0x8, 2, 0);
      }
      __builtin_amdgcn_sched_barrier(0);
    }
    if (wave == 0) TS(1, 3);
#pragma unroll
    for (int s = 0; s < 2; ++s)
#pragma unroll
      for (int j = 0; j < 4; ++j) {
        int t = tm * 128 + 64 * wt + 16 * j + c;
        int d0 = tn * 64 + 32 * wd + 16 * s + 4 * q;
        *(f32x4*)(y + (size_t)t * DM + d0) = acc[s][j];
      }
  }
}

__global__ __launch_bounds__(256) void k0_zero(u32x4* __restrict__ g) {
  g[blockIdx.x * 256 + threadIdx.x] = (u32x4){0u, 0u, 0u, 0u};
}
extern "C" void kernel_launch(void* const* d_in, const int* in_sizes, int n_in, void* d_out, int out_size,
                              void* d_ws, size_t ws_size, hipStream_t stream) {
  const u16* x = (const u16*)d_in[0];
  const u16* lre = (const u16*)d_in[1];
  const u16* lim = (const u16*)d_in[2];
  const u16* bre = (const u16*)d_in[3];
  const u16* bim = (const u16*)d_in[4];
  const u16* cre = (const u16*)d_in[5];
  const u16* cim = (const u16*)d_in[6];
  const u16* ldt = (const u16*)d_in[7];
  const u16* dsk = (const u16*)d_in[8];
  float* y = (float*)d_out;
  u64* gran = (u64*)d_ws;
  u16* hbuf = (u16*)((char*)d_ws + (1u << 20));
  k0_zero<<<64, 256, 0, stream>>>((u32x4*)gran);
  k1_in_scan<<<256, 512, 0, stream>>>(x, lre, lim, bre, bim, ldt, hbuf, gran);
  k2_out<<<256, 512, 0, stream>>>(hbuf, cre, cim, x, dsk, y);
}
```

```cpp
#include <hip/hip_runtime.h>
#include <hip/hip_bf16.h>
#include <stdint.h>

typedef __attribute__((ext_vector_type(8))) short bf16x8;
typedef __attribute__((ext_vector_type(4))) float f32x4;
typedef __attribute__((ext_vector_type(2))) float f32x2;
typedef __attribute__((ext_vector_type(4))) unsigned u32x4;
typedef __attribute__((ext_vector_type(2))) unsigned u32x2;
typedef unsigned short u16;
typedef unsigned long long u64;

#define GAS __attribute__((address_space(1)))
#define LAS __attribute__((address_space(3)))
typedef GAS u64 gu64;

constexpr int SEQ = 2048, DM = 1024, PS = 1024;

template <int N> __device__ __forceinline__ void wait_vm() { asm volatile("s_waitcnt vmcnt(%0)" ::"n"(N) : "memory"); }
__device__ __forceinline__ void bar_lgkm() { asm volatile("s_waitcnt lgkmcnt(0)\n\ts_barrier" ::: "memory"); }
__device__ __forceinline__ void bar_raw() { asm volatile("s_barrier" ::: "memory"); }
__device__ __forceinline__ void wait_lgkm0() { asm volatile("s_waitcnt lgkmcnt(0)" ::: "memory"); }
__device__ __forceinline__ void wait_vm0_visible() {
  asm volatile("" ::: "memory");
  __builtin_amdgcn_s_waitcnt(0x0F70);
  asm volatile("" ::: "memory");
}

__device__ __forceinline__ float bf2f(u16 v) { return __uint_as_float(((unsigned)v) << 16); }
typedef __bf16 bf16x2_t __attribute__((ext_vector_type(2)));
__device__ __forceinline__ unsigned pack_bf16(float lo, float hi) {
  bf16x2_t r = __builtin_convertvector((f32x2){lo, hi}, bf16x2_t);
  return __builtin_bit_cast(unsigned, r);
}
__device__ __forceinline__ float rbf(float v) { return __bfloat162float(__float2bfloat16(v)); }

__device__ __forceinline__ void glds16(const void* g, void* l) {
  __builtin_amdgcn_global_load_lds((const GAS void*)g, (LAS void*)l, 16, 0, 0);
}

#define TS(kern, slot) do {} while (0)
#define TSARG

struct cpx { float r, i; };
__device__ __forceinline__ cpx cmul(cpx a, cpx b) { return {a.r * b.r - a.i * b.i, a.r * b.i + a.i * b.r}; }
__device__ __forceinline__ cpx cfma(cpx a, cpx b, cpx c) {
  return {fmaf(a.r, b.r, fmaf(-a.i, b.i, c.r)), fmaf(a.r, b.i, fmaf(a.i, b.r, c.i))};
}
__device__ __forceinline__ cpx csq(cpx a) { return cmul(a, a); }
struct cpx2 { f32x2 r, i; };
__device__ __forceinline__ f32x2 fma2(f32x2 a, f32x2 b, f32x2 c) { return __builtin_elementwise_fma(a, b, c); }
__device__ __forceinline__ f32x2 splat(float v) { return (f32x2){v, v}; }
__device__ __forceinline__ cpx2 cfma2(f32x2 lr, f32x2 li, f32x2 nli, cpx2 w, f32x2 vr, f32x2 vi) {
  cpx2 o;
  o.r = fma2(lr, w.r, fma2(nli, w.i, vr));
  o.i = fma2(lr, w.i, fma2(li, w.r, vi));
  return o;
}

__device__ __forceinline__ void disc(const u16* lre, const u16* lim, const u16* ldt, int p, cpx& lam, cpx& coef) {
  float lr = bf2f(lre[p]), li = bf2f(lim[p]);
  float dt = rbf(expf(bf2f(ldt[p])));
  float zr = dt * lr, zi = dt * li;
  float e = expf(zr), s, c;
  sincosf(zi, &s, &c);
  lam.r = e * c;
  lam.i = e * s;
  float nr = lam.r - 1.0f, ni = lam.i;
  float den = lr * lr + li * li;
  coef.r = (nr * lr + ni * li) / den;
  coef.i = (ni * lr - nr * li) / den;
}

constexpr int NST = 4;
constexpr int STAGE = 32768;
constexpr int XB = 122880;
constexpr int T_SEG = 131072;
constexpr int TAB = 155648;
constexpr int T_LAM = TAB;
constexpr int T_C2 = TAB + 512;
constexpr int T_PART = TAB + 1024;
constexpr int T_PRE = TAB + 3072;
constexpr int T_CNT = TAB + 7168;
constexpr int T_TS = TAB + 7232;
constexpr int LDS_TOTAL = TAB + 7232 + 256;
constexpr int NKT1 = DM / 64;

#define SGB(m, n) __builtin_amdgcn_sched_group_barrier(m, n, 0)
#define SB0() __builtin_amdgcn_sched_barrier(0)
#define BULK_MODE 0
#define HR 6
#define PACE 1

__global__ __launch_bounds__(512, 2) void s5_fused(const u16* __restrict__ x, const u16* __restrict__ lre,
                                                   const u16* __restrict__ lim, const u16* __restrict__ bre,
                                                   const u16* __restrict__ bim, const u16* __restrict__ cre,
                                                   const u16* __restrict__ cim, const u16* __restrict__ ldt,
                                                   const u16* __restrict__ dsk, float* __restrict__ y,
                                                   u16* __restrict__ hbuf, u64* __restrict__ gran TSARG) {
  __shared__ __attribute__((aligned(1024))) char smem[LDS_TOTAL];
  const int tid = threadIdx.x, lane = tid & 63;
  const int wave = __builtin_amdgcn_readfirstlane(tid >> 6);
  const int b = blockIdx.x, xcd = b & 7, idx = b >> 3;
  const int cm = (xcd >> 1) * 4 + (idx >> 3);
  const int cn = (xcd & 1) * 8 + (idx & 7);
  const int c = lane & 15, q = lane >> 4;
  if (wave == 0) TS(0, 0);

  const char* cb0 = (const char*)cre + (size_t)cn * 64 * 2048;
  const char* cb1 = (const char*)cim + (size_t)cn * 64 * 2048;
  auto cpiece_off = [&](int p) -> unsigned {
    int row = 8 * p + (lane >> 3);
    int chs = (lane & 7) ^ ((row >> 1) & 7);
    return (unsigned)(row * 2048 + chs * 16);
  };
  u32x4 creg[34];
  f32x4 acc2[2][4];
  u32x2 skx[2][4], skd[4];
  const int tq = wave & 3, kh = wave >> 2;
  auto load_skip = [&]() {
#pragma unroll
    for (int s = 0; s < 4; ++s) {
      int d0 = cn * 64 + 16 * s + 4 * q;
      skd[s] = *(const u32x2*)(dsk + d0);
#pragma unroll
      for (int j = 0; j < 2; ++j) skx[j][s] = *(const u32x2*)(x + (size_t)(cm * 128 + 32 * tq + 16 * j + c) * DM + d0);
    }
  };
  auto fin_skip = [&]() {
    asm volatile("" : "+v"(skx[0][0]), "+v"(skx[0][1]), "+v"(skx[0][2]), "+v"(skx[0][3]), "+v"(skd[0]), "+v"(skd[1]), "+v"(skd[2]), "+v"(skd[3]));
    asm volatile("" : "+v"(skx[1][0]), "+v"(skx[1][1]), "+v"(skx[1][2]), "+v"(skx[1][3]));
#pragma unroll
    for (int j = 0; j < 2; ++j)
#pragma unroll
      for (int s = 0; s < 4; ++s) {
        u32x2 xv = skx[j][s], dv = skd[s];
        acc2[j][s][0] = rbf(__uint_as_float(xv[0] << 16) * __uint_as_float(dv[0] << 16));
        acc2[j][s][1] = rbf(__uint_as_float(xv[0] & 0xffff0000u) * __uint_as_float(dv[0] & 0xffff0000u));
        acc2[j][s][2] = rbf(__uint_as_float(xv[1] << 16) * __uint_as_float(dv[1] << 16));
        acc2[j][s][3] = rbf(__uint_as_float(xv[1] & 0xffff0000u) * __uint_as_float(dv[1] & 0xffff0000u));
      }
  };

  gu64* gL = (gu64*)gran + (size_t)cn * 16 * 128;
  gu64* gH = (gu64*)gran + 16 * 16 * 128 + cm * 16 * 16;
  const __amdgpu_buffer_rsrc_t hrs = __builtin_amdgcn_make_buffer_rsrc(hbuf, 0, SEQ * 2048 * 2, 0x00020000);

  if (wave >= 4) {
    const int w = wave - 4;
    unsigned aoff[4], boff[4];
#pragma unroll
    for (int i = 0; i < 4; ++i) {
      int row = 8 * (4 * w + i) + (lane >> 3);
      int chs = (lane & 7) ^ ((row >> 1) & 7);
      int hm = row >> 6, s = (row >> 4) & 3, r = row & 15;
      int t = 64 * hm + 16 * (r >> 2) + 8 * (r & 1) + 2 * s + ((r >> 1) & 1);
      aoff[i] = (unsigned)(t * DM + chs * 8) * 2u;
      int ch = 32 * hm + 2 * r + (s & 1);
      boff[i] = (unsigned)(ch * DM + chs * 8) * 2u;
    }
    const char* abase = (const char*)x + (size_t)cm * 128 * DM * 2;
    const char* bbase = (const char*)((w & 1) ? bim : bre) + (size_t)cn * 64 * DM * 2;
    auto issue = [&](int kt) {
      char* dst = smem + (kt % NST) * STAGE + w * 4096;
#pragma unroll
      for (int i = 0; i < 4; ++i) {
        glds16(abase + kt * 128 + aoff[i], dst + i * 1024);
        glds16(bbase + kt * 128 + boff[i], dst + 16384 + i * 1024);
      }
    };
    const unsigned co0 = cpiece_off(2 * w), co1 = cpiece_off(2 * w + 1);
    auto issue_c = [&](int kt0, int n) {
#pragma unroll
      for (int i = 0; i < n; ++i) {
        int kt = kt0 + i;
        const char* cb = ((kt & 1) ? cb1 : cb0) + (kt >> 1) * 128;
        glds16(cb + co0, smem + kt * 8192 + (2 * w) * 1024);
        glds16(cb + co1, smem + kt * 8192 + (2 * w + 1) * 1024);
      }
    };
#pragma unroll
    for (int kt = 0; kt < NST; ++kt) issue(kt);
    wait_vm<24>();
    bar_raw();
#pragma unroll 1
    for (int kt = 0; kt < NKT1 - NST; ++kt) {
      wait_vm<16>();
      bar_raw();
      issue(kt + NST);
    }
#pragma unroll
    for (int g = 0; g < 3; ++g) {
      wait_vm<16>();
      bar_raw();
      issue_c(4 * g, 4);
    }
    bar_raw();
    issue_c(12, 3);
    load_skip();
#pragma unroll
    for (int i = 0; i < 17; ++i) {
      int kt = 15 + i;
      const char* cb = ((kt & 1) ? cb1 : cb0) + (kt >> 1) * 128;
      creg[2 * i] = *(const u32x4*)(cb + co0);
      creg[2 * i + 1] = *(const u32x4*)(cb + co1);
      if (PACE) asm volatile("s_sleep %0" ::"n"(PACE) : "memory");
    }
    if (wave == 4) TS(0, 16);
    bar_raw();
    wait_vm0_visible();
    if (wave == 4) TS(0, 17);
    fin_skip();
#pragma unroll
    for (int i = 0; i < 4; ++i) {
      *(u32x4*)(smem + (15 + i) * 8192 + (2 * w) * 1024 + lane * 16) = creg[2 * i];
      *(u32x4*)(smem + (15 + i) * 8192 + (2 * w + 1) * 1024 + lane * 16) = creg[2 * i + 1];
    }
  } else {
    const int wm = wave >> 1, wn = wave & 1;
    if (wave == 0) {
      cpx lam, cf;
      disc(lre, lim, ldt, cn * 64 + lane, lam, cf);
      *(cpx*)(smem + T_LAM + lane * 8) = lam;
      *(cpx*)(smem + T_C2 + lane * 8) = cpx{2.f * cf.r, 2.f * cf.i};
      if (lane == 0) *(unsigned*)(smem + T_CNT) = 0u;
    }
    const int f = (c >> 1) & 7;
    const unsigned a_rd0 = (unsigned)((64 * wm + c) * 128 + ((q ^ f) << 4));
    const unsigned b_rd0 = (unsigned)(16384 + (64 * wn + c) * 128 + ((q ^ f) << 4));
    f32x4 acc[4][4];
#pragma unroll
    for (int i = 0; i < 4; ++i)
#pragma unroll
      for (int j = 0; j < 4; ++j) acc[i][j] = (f32x4){0.f, 0.f, 0.f, 0.f};
    bf16x8 fa0[4], fb0[4], fa1[4], fb1[4];
    auto rd = [&](bf16x8 (&fa)[4], bf16x8 (&fb)[4], int kt, int ks) {
      const char* base = smem + (kt % NST) * STAGE;
      unsigned ao = a_rd0 ^ (ks << 6), bo = b_rd0 ^ (ks << 6);
#pragma unroll
      for (int i = 0; i < 4; ++i) {
        fa[i] = *(const bf16x8*)(base + ao + i * 2048);
        fb[i] = *(const bf16x8*)(base + bo + i * 2048);
      }
    };
    auto mm = [&](bf16x8 (&fa)[4], bf16x8 (&fb)[4]) {
#pragma unroll
      for (int i = 0; i < 4; ++i)
#pragma unroll
        for (int j = 0; j < 4; ++j)
          acc[i][j] = __builtin_amdgcn_mfma_f32_16x16x32_bf16(fa[i], fb[j], acc[i][j], 0, 0, 0);
    };
    if (wave == 0) TS(0, 1);
    bar_lgkm();
    if (wave == 0) TS(0, 2);
    rd(fa0, fb0, 0, 0);
    const int chb = (32 * wn + 2 * c) * 8;
    const int g = 4 * wm + q;
    f32x4 lam4 = *(const f32x4*)(smem + T_LAM + chb);
    f32x4 c24 = *(const f32x4*)(smem + T_C2 + chb);
    cpx l8A, l8B, pA, pB, l16c, l128c, l512c;
    {
      cpx lA = {lam4[0], lam4[1]}, lB = {lam4[2], lam4[3]};
      l8A = csq(csq(csq(lA))); l8B = csq(csq(csq(lB)));
      cpx eA = csq(l8A), eB = csq(l8B);
      pA = cpx{1.f, 0.f}; pB = cpx{1.f, 0.f};
#pragma unroll
      for (int bit = 0; bit < 3; ++bit) {
        cpx mA = cmul(pA, eA), mB = cmul(pB, eB);
        bool on = (g >> bit) & 1;
        pA.r = on ? mA.r : pA.r; pA.i = on ? mA.i : pA.i;
        pB.r = on ? mB.r : pB.r; pB.i = on ? mB.i : pB.i;
        if (bit < 2) { eA = csq(eA); eB = csq(eB); }
      }
      cpx lc = *(const cpx*)(smem + T_LAM + lane * 8);
      l16c = csq(csq(csq(csq(lc))));
      l128c = csq(csq(csq(l16c)));
      l512c = csq(csq(l128c));
      asm volatile("" : "+v"(l8A.r), "+v"(l8A.i), "+v"(l8B.r), "+v"(l8B.i), "+v"(pA.r), "+v"(pA.i), "+v"(pB.r), "+v"(pB.i));
      asm volatile("" : "+v"(l16c.r), "+v"(l16c.i), "+v"(l128c.r), "+v"(l128c.i), "+v"(l512c.r), "+v"(l512c.i), "+v"(lam4), "+v"(c24));
    }
    SB0();
#pragma unroll
    for (int kt = 0; kt < NKT1; ++kt) {
      rd(fa1, fb1, kt, 1);
      mm(fa0, fb0);
#pragma unroll
      for (int i = 0; i < 8; ++i) { SGB(0x100, 1); SGB(0x8, 1); }
      SGB(0x8, 8);
      SB0();
      if (kt < NKT1 - 1) {
        bar_lgkm();
        rd(fa0, fb0, kt + 1, 0);
      }
      mm(fa1, fb1);
      if (kt < NKT1 - 1) {
#pragma unroll
        for (int i = 0; i < 8; ++i) { SGB(0x100, 1); SGB(0x8, 1); }
        SGB(0x8, 8);
      }
      SB0();
    }
    if (wave == 0) TS(0, 3);
    const f32x2 lrA = splat(lam4[0]), liA = splat(lam4[1]), nliA = splat(-lam4[1]);
    const f32x2 lrB = splat(lam4[2]), liB = splat(lam4[3]), nliB = splat(-lam4[3]);
    cpx2 aA = {splat(0.f), splat(0.f)}, aB = {splat(0.f), splat(0.f)};
#pragma unroll
    for (int s = 0; s < 4; ++s) {
      aA = cfma2(lrA, liA, nliA, aA, acc[s][0].xy, acc[s][2].xy);
      aB = cfma2(lrB, liB, nliB, aB, acc[s][1].xy, acc[s][3].xy);
      aA = cfma2(lrA, liA, nliA, aA, acc[s][0].zw, acc[s][2].zw);
      aB = cfma2(lrB, liB, nliB, aB, acc[s][1].zw, acc[s][3].zw);
    }
    const cpx a0A = {aA.r.x, aA.i.x}, a0B = {aB.r.x, aB.i.x};
    {
      cpx sA = cfma(l8A, a0A, cpx{aA.r.y, aA.i.y});
      cpx sB = cfma(l8B, a0B, cpx{aB.r.y, aB.i.y});
      f32x4 t4 = {sA.r, sA.i, sB.r, sB.i};
      *(f32x4*)(smem + T_SEG + g * 512 + chb) = t4;
    }
    bar_lgkm();
    if (wave == 0) TS(0, 4);
    if (wave == 0) {
      cpx sg[8];
#pragma unroll
      for (int gg = 0; gg < 8; ++gg) sg[gg] = *(const cpx*)(smem + T_SEG + (gg * 64 + lane) * 8);
      cpx pre[8];
      cpx a = {0.f, 0.f};
#pragma unroll
      for (int gg = 0; gg < 8; ++gg) { pre[gg] = a; a = cfma(l16c, a, sg[gg]); }
      if (cm < 15) {
        gu64* gp = gL + cm * 128 + lane * 2;
        __hip_atomic_store(gp, (1ull << 32) | (u64)__float_as_uint(a.r), __ATOMIC_RELAXED, __HIP_MEMORY_SCOPE_AGENT);
        __hip_atomic_store(gp + 1, (1ull << 32) | (u64)__float_as_uint(a.i), __ATOMIC_RELAXED, __HIP_MEMORY_SCOPE_AGENT);
      }
#pragma unroll
      for (int gg = 0; gg < 8; ++gg) *(cpx*)(smem + T_PRE + (gg * 64 + lane) * 8) = pre[gg];
    }
    if (wave == 0) TS(0, 11);
    const cpx c2A = {c24[0], c24[1]}, c2B = {c24[2], c24[3]};
#pragma unroll
    for (int s = 0; s < 4; ++s) {
      f32x4 vr = acc[s][0], vi = acc[s][2];
      acc[s][0] = vr * c2A.r - vi * c2A.i;
      acc[s][2] = vi * c2A.r + vr * c2A.i;
      vr = acc[s][1]; vi = acc[s][3];
      acc[s][1] = vr * c2B.r - vi * c2B.i;
      acc[s][3] = vi * c2B.r + vr * c2B.i;
      asm volatile("" : "+v"(acc[s][0]), "+v"(acc[s][1]), "+v"(acc[s][2]), "+v"(acc[s][3]));
    }
    if (wave == 0) TS(0, 12);
    {
      const int w = wave;
      cpx part = {0.f, 0.f};
      if (w < cm) {
        const cpx l128 = l128c, l512 = l512c;
        const int nj = (cm - w + 3) >> 2;
        u64 v[4][2];
        for (unsigned spins = 0;; ++spins) {
          bool ok = true;
#pragma unroll
          for (int i = 0; i < 4; ++i) {
            if (i < nj) {
              gu64* gp = gL + (w + 4 * i) * 128 + lane * 2;
              v[i][0] = __hip_atomic_load(gp, __ATOMIC_RELAXED, __HIP_MEMORY_SCOPE_AGENT);
              v[i][1] = __hip_atomic_load(gp + 1, __ATOMIC_RELAXED, __HIP_MEMORY_SCOPE_AGENT);
            }
          }
#pragma unroll
          for (int i = 0; i < 4; ++i)
            if (i < nj) ok = ok && ((v[i][0] >> 32) == 1ull) && ((v[i][1] >> 32) == 1ull);
          if (__all(ok)) break;
          if (spins > (1u << 22)) break;
        }
        cpx a = {0.f, 0.f};
#pragma unroll
        for (int i = 0; i < 4; ++i) {
          if (i < nj) {
            cpx aj = {__uint_as_float((unsigned)v[i][0]), __uint_as_float((unsigned)v[i][1])};
            a = cfma(l512, a, aj);
          }
        }
        int e0 = cm - 1 - (w + 4 * (nj - 1));
        for (int e = 0; e < e0; ++e) a = cmul(a, l128);
        part = a;
      }
      *(cpx*)(smem + T_PART + (w * 64 + lane) * 8) = part;
    }
    if (wave == 0) TS(0, 13);
    {
      wait_lgkm0();
      LAS unsigned* cnt = (LAS unsigned*)(smem + T_CNT);
      if (lane == 0) __hip_atomic_fetch_add(cnt, 1u, __ATOMIC_RELAXED, __HIP_MEMORY_SCOPE_WORKGROUP);
      while (__hip_atomic_load(cnt, __ATOMIC_RELAXED, __HIP_MEMORY_SCOPE_WORKGROUP) < 4u) {}
      asm volatile("" ::: "memory");
    }
    if (wave == 0) TS(0, 5);
    cpx wA, wB;
    {
      f32x4 t0 = *(const f32x4*)(smem + T_PART + chb), t1 = *(const f32x4*)(smem + T_PART + 512 + chb);
      f32x4 t2 = *(const f32x4*)(smem + T_PART + 1024 + chb), t3 = *(const f32x4*)(smem + T_PART + 1536 + chb);
      f32x4 pr = *(const f32x4*)(smem + T_PRE + g * 512 + chb);
      f32x4 cs = (t0 + t1) + (t2 + t3);
      wA = cfma(pA, cpx{cs[0], cs[1]}, cpx{pr[0], pr[1]});
      wB = cfma(pB, cpx{cs[2], cs[3]}, cpx{pr[2], pr[3]});
    }
    cpx2 uA, uB;
    {
      cpx w1A = cfma(l8A, wA, a0A), w1B = cfma(l8B, wB, a0B);
      cpx u0A = cmul(c2A, wA), u1A = cmul(c2A, w1A), u0B = cmul(c2B, wB), u1B = cmul(c2B, w1B);
      uA.r = (f32x2){u0A.r, u1A.r}; uA.i = (f32x2){u0A.i, u1A.i};
      uB.r = (f32x2){u0B.r, u1B.r}; uB.i = (f32x2){u0B.i, u1B.i};
    }
    const unsigned swz = (unsigned)((c >> 2) | ((q & 1) << 2));
    char* obase = smem + XB + (g * 4 + wn) * 1024 + (c >> 2) * 256 + (c & 3) * 4;
#pragma unroll
    for (int s = 0; s < 4; ++s)
#pragma unroll
      for (int rh = 0; rh < 2; ++rh) {
        if (rh == 0) {
          uA = cfma2(lrA, liA, nliA, uA, acc[s][0].xy, acc[s][2].xy);
          uB = cfma2(lrB, liB, nliB, uB, acc[s][1].xy, acc[s][3].xy);
        } else {
          uA = cfma2(lrA, liA, nliA, uA, acc[s][0].zw, acc[s][2].zw);
          uB = cfma2(lrB, liB, nliB, uB, acc[s][1].zw, acc[s][3].zw);
        }
        unsigned o = (((unsigned)(2 * s + rh)) ^ swz) * 16;
        *(unsigned*)(obase + o) = pack_bf16(uA.r.x, uB.r.x);
        *(unsigned*)(obase + o + 128) = pack_bf16(uA.r.y, uB.r.y);
        *(unsigned*)(obase + o + 2048) = pack_bf16(-uA.i.x, -uB.i.x);
        *(unsigned*)(obase + o + 2048 + 128) = pack_bf16(-uA.i.y, -uB.i.y);
      }
    wait_lgkm0();
    if (wave == 0) TS(0, 6);
#pragma unroll
    for (int qq = 0; qq < 4; ++qq) {
      const unsigned rs = (unsigned)(lane ^ ((lane >> 4) | ((qq & 1) << 2))) * 16;
      const int gq = 4 * wm + qq;
#pragma unroll
      for (int pp = 0; pp < 2; ++pp) {
        u32x4 v = *(const u32x4*)(smem + XB + (gq * 4 + wn + 2 * pp) * 1024 + rs);
        __builtin_amdgcn_raw_buffer_store_b128(v, hrs, (unsigned)((cm * 8 + gq) * 65536 + (cn * 4 + wn + 2 * pp) * 1024 + lane * 16), 0, 16);
      }
    }
    if (wave == 0) TS(0, 14);
    wait_vm0_visible();
    if (wave == 0) TS(0, 15);
    bar_lgkm();
    if (wave == 0) TS(0, 7);
    if (wave == 0) {
      if (lane == 0) __hip_atomic_store(gH + cn * 16, 1ull << 32, __ATOMIC_RELAXED, __HIP_MEMORY_SCOPE_AGENT);
      for (unsigned spins = 0;; ++spins) {
        u64 v = __hip_atomic_load(gH + (lane & 15) * 16, __ATOMIC_RELAXED, __HIP_MEMORY_SCOPE_AGENT);
        if (__all((v >> 32) == 1ull)) break;
        if (spins > (1u << 22)) break;
      }
    }
#pragma unroll
    for (int j = 0; j < 2; ++j)
#pragma unroll
      for (int s = 0; s < 4; ++s) acc2[j][s] = (f32x4){0.f, 0.f, 0.f, 0.f};
  }
  wait_vm0_visible();
  bar_lgkm();
  if (wave == 0) TS(0, 8);

  {
    const unsigned hwave = (unsigned)((cm * 8 + 2 * tq) * 65536 + kh * 2048 + lane * 16);
    u32x4 hr[HR][2];
    const unsigned c_rd0 = (unsigned)(c * 128 + ((q ^ ((c >> 1) & 7)) << 4));
    bf16x8 cf[2][4];
    unsigned cbase[3][2], cwrap[2];
#pragma unroll
    for (int r = 0; r < 3; ++r)
#pragma unroll
      for (int p = 0; p < 2; ++p) {
        cbase[r][p] = (c_rd0 ^ (p << 6)) + r * 65536 + kh * 8192;
        asm volatile("" : "+v"(cbase[r][p]));
      }
#pragma unroll
    for (int p = 0; p < 2; ++p) {
      cwrap[p] = (c_rd0 ^ (p << 6)) + (kh ? 0 : 18 * 8192);
      asm volatile("" : "+v"(cwrap[p]));
    }
    auto rdc = [&](bf16x8 (&dst)[4], int n) {
      const int ke = 2 * (n >> 1);
      const char* o;
      int off;
      if (ke == 18) { o = smem + cwrap[n & 1]; off = 0; }
      else {
        const int slot = ke < 18 ? ke : ke - 19;
        o = smem + cbase[slot >> 3][n & 1]; off = (slot & 7) * 8192;
      }
#pragma unroll
      for (int s = 0; s < 4; ++s) dst[s] = *(const bf16x8*)(o + off + s * 2048);
    };
    auto ldh = [&](int n, int j) -> u32x4 {
      return __builtin_amdgcn_raw_buffer_load_b128(hrs, hwave + j * 65536 + (n >> 1) * 4096 + (n & 1) * 1024, 0, 16);
    };
    rdc(cf[0], 0);
    asm volatile("" ::"v"(cf[0][0]), "v"(cf[0][1]), "v"(cf[0][2]), "v"(cf[0][3]) : "memory");
#pragma unroll
    for (int i = 0; i < HR; ++i) {
      hr[i][0] = ldh(i, 0);
      hr[i][1] = ldh(i, 1);
      SB0();
    }
#pragma unroll
    for (int n = 0; n < 32; ++n) {
      if (n == 6 || n == 14) {
        bar_lgkm();
        if (wave >= 4) {
#pragma unroll
          for (int i = (n == 6 ? 0 : 6); i < (n == 6 ? 6 : 13); ++i) {
            *(u32x4*)(smem + i * 8192 + (2 * (wave - 4)) * 1024 + lane * 16) = creg[8 + 2 * i];
            *(u32x4*)(smem + i * 8192 + (2 * (wave - 4) + 1) * 1024 + lane * 16) = creg[8 + 2 * i + 1];
          }
        }
        SB0();
      }
      if (n == 23) { bar_lgkm(); SB0(); }
      if (n < 31) rdc(cf[(n + 1) & 1], n + 1);
      bf16x8 h0 = __builtin_bit_cast(bf16x8, hr[n % HR][0]), h1 = __builtin_bit_cast(bf16x8, hr[n % HR][1]);
#pragma unroll
      for (int s = 0; s < 4; ++s) {
        acc2[0][s] = __builtin_amdgcn_mfma_f32_16x16x32_bf16(cf[n & 1][s], h0, acc2[0][s], 0, 0, 0);
        acc2[1][s] = __builtin_amdgcn_mfma_f32_16x16x32_bf16(cf[n & 1][s], h1, acc2[1][s], 0, 0, 0);
      }
      if (n < 32 - HR) { hr[n % HR][0] = ldh(n + HR, 0); hr[n % HR][1] = ldh(n + HR, 1); }
      if (n == 0 && wave == 0) TS(0, 20);
      if (n == 13 && wave == 0) TS(0, 21);
      if (n == 14 && wave == 0) TS(0, 22);
      if (n == 17 && wave == 0) TS(0, 23);
      if (n == 13 && wave == 4) TS(0, 24);
      if (n == 14 && wave == 4) TS(0, 25);
      if (n < 31) { SGB(0x100, 4); }
      SGB(0x8, 8);
      if (n < 32 - HR) SGB(0x20, 2);
      SB0();
    }
    if (wave == 0) TS(0, 9);
    if (wave == 4) TS(0, 18);
    char* xw = smem + 13 * 8192 + wave * 4096 + lane * 16;
    char* xr = smem + 13 * 8192 + (wave ^ 4) * 4096 + lane * 16;
#pragma unroll
    for (int s = 0; s < 4; ++s) *(f32x4*)(xw + s * 1024) = kh ? acc2[0][s] : acc2[1][s];
    bar_lgkm();
    if (wave == 0) TS(0, 19);
    const int t = cm * 128 + 32 * tq + 16 * kh + c;
#pragma unroll
    for (int s = 0; s < 4; ++s) {
      f32x4 o = *(const f32x4*)(xr + s * 1024);
      f32x4 mine = kh ? acc2[1][s] : acc2[0][s];
      *(f32x4*)(y + (size_t)t * DM + cn * 64 + 16 * s + 4 * q) = mine + o;
    }
  }
}

extern "C" void kernel_launch(void* const* d_in, const int* in_sizes, int n_in, void* d_out, int out_size,
                              void* d_ws, size_t ws_size, hipStream_t stream) {
  const u16* x = (const u16*)d_in[0];
  const u16* lre = (const u16*)d_in[1];
  const u16* lim = (const u16*)d_in[2];
  const u16* bre = (const u16*)d_in[3];
  const u16* bim = (const u16*)d_in[4];
  const u16* cre = (const u16*)d_in[5];
  const u16* cim = (const u16*)d_in[6];
  const u16* ldt = (const u16*)d_in[7];
  const u16* dsk = (const u16*)d_in[8];
  float* y = (float*)d_out;
  u64* gran = (u64*)d_ws;
  u16* hbuf = (u16*)((char*)d_ws + (1u << 20));
  (void)hipMemsetAsync(gran, 0, (16 * 16 * 128 + 16 * 16 * 16) * 8, stream);
  s5_fused<<<256, 512, 0, stream>>>(x, lre, lim, bre, bim, cre, cim, ldt, dsk, y, hbuf, gran);
}
```

```cpp
#include <hip/hip_runtime.h>
#include <hip/hip_bf16.h>
#include <stdint.h>

typedef __attribute__((ext_vector_type(8))) short bf16x8;
typedef __attribute__((ext_vector_type(4))) float f32x4;
typedef __attribute__((ext_vector_type(2))) float f32x2;
typedef __attribute__((ext_vector_type(4))) unsigned u32x4;
typedef __attribute__((ext_vector_type(2))) unsigned u32x2;
typedef unsigned short u16;
typedef unsigned long long u64;

#define GAS __attribute__((address_space(1)))
#define LAS __attribute__((address_space(3)))
typedef GAS u64 gu64;

constexpr int SEQ = 2048, DM = 1024, PS = 1024;

template <int N> __device__ __forceinline__ void wait_vm() { asm volatile("s_waitcnt vmcnt(%0)" ::"n"(N) : "memory"); }
__device__ __forceinline__ void bar_lgkm() { asm volatile("s_waitcnt lgkmcnt(0)\n\ts_barrier" ::: "memory"); }
__device__ __forceinline__ void bar_raw() { asm volatile("s_barrier" ::: "memory"); }
__device__ __forceinline__ void wait_lgkm0() { asm volatile("s_waitcnt lgkmcnt(0)" ::: "memory"); }
__device__ __forceinline__ void wait_vm0_visible() {
  asm volatile("" ::: "memory");
  __builtin_amdgcn_s_waitcnt(0x0F70);
  asm volatile("" ::: "memory");
}

__device__ __forceinline__ float bf2f(u16 v) { return __uint_as_float(((unsigned)v) << 16); }
typedef __bf16 bf16x2_t __attribute__((ext_vector_type(2)));
__device__ __forceinline__ unsigned pack_bf16(float lo, float hi) {
  bf16x2_t r = __builtin_convertvector((f32x2){lo, hi}, bf16x2_t);
  return __builtin_bit_cast(unsigned, r);
}
__device__ __forceinline__ float rbf(float v) { return __bfloat162float(__float2bfloat16(v)); }

__device__ __forceinline__ void glds16(const void* g, void* l) {
  __builtin_amdgcn_global_load_lds((const GAS void*)g, (LAS void*)l, 16, 0, 0);
}


struct cpx { float r, i; };
__device__ __forceinline__ cpx cmul(cpx a, cpx b) { return {a.r * b.r - a.i * b.i, a.r * b.i + a.i * b.r}; }
__device__ __forceinline__ cpx cfma(cpx a, cpx b, cpx c) {
  return {fmaf(a.r, b.r, fmaf(-a.i, b.i, c.r)), fmaf(a.r, b.i, fmaf(a.i, b.r, c.i))};
}
__device__ __forceinline__ cpx csq(cpx a) { return cmul(a, a); }
struct cpx2 { f32x2 r, i; };
__device__ __forceinline__ f32x2 fma2(f32x2 a, f32x2 b, f32x2 c) { return __builtin_elementwise_fma(a, b, c); }
__device__ __forceinline__ f32x2 splat(float v) { return (f32x2){v, v}; }
__device__ __forceinline__ cpx2 cfma2(f32x2 lr, f32x2 li, f32x2 nli, cpx2 w, f32x2 vr, f32x2 vi) {
  cpx2 o;
  o.r = fma2(lr, w.r, fma2(nli, w.i, vr));
  o.i = fma2(lr, w.i, fma2(li, w.r, vi));
  return o;
}

__device__ __forceinline__ void disc(u16 lre_, u16 lim_, u16 ldt_, cpx& lam, cpx& coef) {
  float lr = bf2f(lre_), li = bf2f(lim_);
  float dt = rbf(expf(bf2f(ldt_)));
  float zr = dt * lr, zi = dt * li;
  float e = expf(zr), s, c;
  sincosf(zi, &s, &c);
  lam.r = e * c;
  lam.i = e * s;
  float nr = lam.r - 1.0f, ni = lam.i;
  float den = lr * lr + li * li;
  coef.r = (nr * lr + ni * li) / den;
  coef.i = (ni * lr - nr * li) / den;
}

constexpr int NST = 4;
constexpr int STAGE = 32768;
constexpr int XB = 122880;
constexpr int T_SEG = 131072;
constexpr int TAB = 155648;
constexpr int T_LAM = TAB;
constexpr int T_C2 = TAB + 512;
constexpr int T_PART = TAB + 1024;
constexpr int T_PRE = TAB + 3072;
constexpr int T_CNT = TAB + 7168;
constexpr int T_PROT = TAB + 7184;
constexpr int T_SIN = TAB + 7232;
constexpr int LDS_TOTAL = TAB + 7872 + 256;
constexpr unsigned WS_RQ = 0;
constexpr unsigned WS_AK = 32768;
constexpr unsigned WS_IN = 65536;
constexpr unsigned WS_OL = 131072;
constexpr unsigned WS_PROT_BYTES = WS_OL + 256 * 1024;
constexpr int NKT1 = DM / 64;

constexpr int HR = 6;
#define SGB(m, n) __builtin_amdgcn_sched_group_barrier(m, n, 0)
#define SB0() __builtin_amdgcn_sched_barrier(0)

__global__ __launch_bounds__(512, 2) void s5_fused(const u16* __restrict__ x, const u16* __restrict__ lre,
                                                   const u16* __restrict__ lim, const u16* __restrict__ bre,
                                                   const u16* __restrict__ bim, const u16* __restrict__ cre,
                                                   const u16* __restrict__ cim, const u16* __restrict__ ldt,
                                                   const u16* __restrict__ dsk, float* __restrict__ y,
                                                   u16* __restrict__ hbuf, u64* __restrict__ gran) {
  __shared__ __attribute__((aligned(1024))) char smem[LDS_TOTAL];
  const int tid = threadIdx.x, lane = tid & 63;
  const int wave = __builtin_amdgcn_readfirstlane(tid >> 6);
  const int b = blockIdx.x, xcd = b & 7, idx = b >> 3;
  const int cm = (xcd >> 1) * 4 + (idx >> 3);
  const int cn = (xcd & 1) * 8 + (idx & 7);
  const int c = lane & 15, q = lane >> 4;

  const char* cb0 = (const char*)cre + (size_t)cn * 64 * 2048;
  const char* cb1 = (const char*)cim + (size_t)cn * 64 * 2048;
  auto cpiece_off = [&](int p) -> unsigned {
    int row = 8 * p + (lane >> 3);
    int chs = (lane & 7) ^ ((row >> 1) & 7);
    return (unsigned)(row * 2048 + chs * 16);
  };
  u32x4 creg[34];
  f32x4 acc2[2][4];
  u32x2 skx[2][4], skd[4];
  const int tq = wave & 3, kh = wave >> 2;
  auto load_skip = [&]() {
#pragma unroll
    for (int s = 0; s < 4; ++s) {
      int d0 = cn * 64 + 16 * s + 4 * q;
      skd[s] = *(const u32x2*)(dsk + d0);
#pragma unroll
      for (int j = 0; j < 2; ++j) skx[j][s] = *(const u32x2*)(x + (size_t)(cm * 128 + 32 * tq + 16 * j + c) * DM + d0);
    }
  };
  auto fin_skip = [&]() {
    asm volatile("" : "+v"(skx[0][0]), "+v"(skx[0][1]), "+v"(skx[0][2]), "+v"(skx[0][3]), "+v"(skd[0]), "+v"(skd[1]), "+v"(skd[2]), "+v"(skd[3]));
    asm volatile("" : "+v"(skx[1][0]), "+v"(skx[1][1]), "+v"(skx[1][2]), "+v"(skx[1][3]));
#pragma unroll
    for (int j = 0; j < 2; ++j)
#pragma unroll
      for (int s = 0; s < 4; ++s) {
        u32x2 xv = skx[j][s], dv = skd[s];
        acc2[j][s][0] = rbf(__uint_as_float(xv[0] << 16) * __uint_as_float(dv[0] << 16));
        acc2[j][s][1] = rbf(__uint_as_float(xv[0] & 0xffff0000u) * __uint_as_float(dv[0] & 0xffff0000u));
        acc2[j][s][2] = rbf(__uint_as_float(xv[1] << 16) * __uint_as_float(dv[1] << 16));
        acc2[j][s][3] = rbf(__uint_as_float(xv[1] & 0xffff0000u) * __uint_as_float(dv[1] & 0xffff0000u));
      }
  };

  const int gid = cm * 16 + cn;
  GAS char* wsb = (GAS char*)gran;
  auto rq_ptr = [&](int g_, int w_) { return (GAS unsigned*)(wsb + WS_RQ + g_ * 128 + w_ * 4); };
  auto ak_ptr = [&](int g_, int i_) { return (GAS unsigned*)(wsb + WS_AK + g_ * 128 + i_ * 4); };
  auto in_ptr = [&](int g_, int i_) { return (gu64*)(wsb + WS_IN + g_ * 256 + i_ * 8); };
  const int l5 = lane & 31;
  const int peer = (l5 < 16) ? (l5 * 16 + cn) : (cm * 16 + l5 - 16);
  const int myslot = (l5 < 16) ? cm : (16 + cn);
  const bool is_dst = (l5 < 16) ? (l5 > cm) : ((l5 - 16) != cn);
  const bool is_src = (l5 < 16) ? (l5 < cm) : ((l5 - 16) != cn);
  const __amdgpu_buffer_rsrc_t prs = __builtin_amdgcn_make_buffer_rsrc(gran, 0, WS_PROT_BYTES, 0x00020000);
#define RLX __ATOMIC_RELAXED, __HIP_MEMORY_SCOPE_AGENT
  const __amdgpu_buffer_rsrc_t hrs = __builtin_amdgcn_make_buffer_rsrc(hbuf, 0, SEQ * 2048 * 2, 0x00020000);

  auto service = [&]() {
    const unsigned S = *(const unsigned*)(smem + T_PROT + 4);
    unsigned ak = __hip_atomic_load(ak_ptr(gid, l5), RLX);
    unsigned r = __hip_atomic_load(rq_ptr(peer, 0), RLX);
    if (lane < 32 && is_dst && ak != S) __hip_atomic_store(in_ptr(peer, myslot), ((u64)r << 32) | S, RLX);
  };

  if (wave >= 4) {
    const int w = wave - 4;
    unsigned aoff[4], boff[4];
#pragma unroll
    for (int i = 0; i < 4; ++i) {
      int row = 8 * (4 * w + i) + (lane >> 3);
      int chs = (lane & 7) ^ ((row >> 1) & 7);
      int hm = row >> 6, s = (row >> 4) & 3, r = row & 15;
      int t = 64 * hm + 16 * (r >> 2) + 8 * (r & 1) + 2 * s + ((r >> 1) & 1);
      aoff[i] = (unsigned)(t * DM + chs * 8) * 2u;
      int ch = 32 * hm + 2 * r + (s & 1);
      boff[i] = (unsigned)(ch * DM + chs * 8) * 2u;
    }
    const char* abase = (const char*)x + (size_t)cm * 128 * DM * 2;
    const char* bbase = (const char*)((w & 1) ? bim : bre) + (size_t)cn * 64 * DM * 2;
    auto issue = [&](int kt) {
      char* dst = smem + (kt % NST) * STAGE + w * 4096;
#pragma unroll
      for (int i = 0; i < 4; ++i) {
        glds16(abase + kt * 128 + aoff[i], dst + i * 1024);
        glds16(bbase + kt * 128 + boff[i], dst + 16384 + i * 1024);
      }
    };
    const unsigned co0 = cpiece_off(2 * w), co1 = cpiece_off(2 * w + 1);
    auto issue_c = [&](int kt0, int n) {
#pragma unroll
      for (int i = 0; i < n; ++i) {
        int kt = kt0 + i;
        const char* cb = ((kt & 1) ? cb1 : cb0) + (((kt >> 1) + cn) & 15) * 128;
        glds16(cb + co0, smem + kt * 8192 + (2 * w) * 1024);
        glds16(cb + co1, smem + kt * 8192 + (2 * w + 1) * 1024);
      }
    };
    u32x4 iol;
    u64 iin, irq;
    unsigned iak;
    {
      GAS char* p0 = wsb + WS_OL + gid * 1024 + lane * 16;
      gu64* p1 = in_ptr(gid, l5);
      GAS unsigned* p2 = rq_ptr(gid, 0);
      GAS unsigned* p3 = ak_ptr(gid, l5);
      asm volatile("global_load_dwordx4 %0, %1, off sc1" : "=v"(iol) : "v"(p0) : "memory");
      asm volatile("global_load_dwordx2 %0, %1, off sc1" : "=v"(iin) : "v"(p1) : "memory");
      asm volatile("global_load_dwordx2 %0, %1, off sc1" : "=v"(irq) : "v"(p2) : "memory");
      asm volatile("global_load_dword %0, %1, off sc1" : "=v"(iak) : "v"(p3) : "memory");
    }
    issue(0);
    issue(1);
    wait_vm<8>();
    bar_raw();
    issue(2);
    issue(3);
    unsigned rq = 0, myT = 0, myS = 0, tchc = 0;
    u64 inv = 0;
#pragma unroll
    for (int kt = 0; kt < NKT1 - NST; ++kt) {
      if (kt == 0 && w == 0) {
        asm volatile("" : "+v"(iol), "+v"(iin), "+v"(irq), "+v"(iak));
        unsigned T = __builtin_amdgcn_readfirstlane((unsigned)irq) + 1u;
        while (__any((unsigned)(iin >> 32) == T)) ++T;
        unsigned S = __builtin_amdgcn_readfirstlane((unsigned)(irq >> 32)) + 1u;
        while (__any(iol[1] == S || iol[3] == S || iak == S)) ++S;
        myT = T; myS = S;
        if (lane == 0) {
          __hip_atomic_store(rq_ptr(gid, 0), T, RLX);
          *(u32x2*)(smem + T_PROT) = (u32x2){T, S};
        }
        wait_lgkm0();
        wait_vm<17>();
      } else {
        wait_vm<16>();
      }
      bar_raw();
      issue(kt + NST);
      if (kt == 4 && (w == 2 || w == 3)) {
        const int xp = xcd >> 1, xo = xcd & 1;
        int li = (idx * 2 + (w - 2)) * 64 + lane;
        int row = xp * 128 + (li >> 5), l5_ = li & 31;
        const char* tp = (const char*)((l5_ >> 4) ? cim : cre) + (size_t)(xo * 512 + row) * 2048 + (l5_ & 15) * 128;
        asm volatile("global_load_dword %0, %1, off" : "=v"(tchc) : "v"(tp) : "memory");
      }
      if (w == 0) {
        if (kt == 6) {
          GAS unsigned* rp = rq_ptr(peer, 0);
          asm volatile("global_load_dword %0, %1, off sc1" : "=v"(rq) : "v"(rp) : "memory");
        }
        if (kt == 9) {
          asm volatile("" : "+v"(rq));
          if (lane < 32 && is_dst) __hip_atomic_store(in_ptr(peer, myslot), ((u64)rq << 32) | myS, RLX);
        }
      }
    }
#pragma unroll
    for (int g = 0; g < 3; ++g) {
      wait_vm<16>();
      bar_raw();
      issue_c(4 * g, 4);
      if (g == 0 && w == 0) {
        gu64* p1 = in_ptr(gid, l5);
        asm volatile("global_load_dwordx2 %0, %1, off sc1" : "=v"(inv) : "v"(p1) : "memory");
      }
    }
    bar_raw();
    if (w == 0) {
      wait_vm<16>();
      asm volatile("" : "+v"(inv));
      for (unsigned spins = 0; !__all(!is_src || (unsigned)(inv >> 32) == myT); ++spins) {
        if (spins > (1u << 22)) break;
        if ((spins & 7u) == 7u) service();
        inv = __hip_atomic_load(in_ptr(gid, l5), RLX);
      }
      {
        const unsigned a0 = (unsigned)(uintptr_t)(LAS char*)(smem + T_SIN) + l5 * 4;
        const unsigned a1 = (unsigned)(uintptr_t)(LAS char*)(smem + T_CNT + 8);
        const unsigned one = 1u, val = (unsigned)inv;
        asm volatile("ds_write_b32 %0, %1\n\ts_waitcnt lgkmcnt(0)\n\tds_write_b32 %2, %3" ::"v"(a0), "v"(val), "v"(a1), "v"(one) : "memory");
      }
      if (lane < 32 && is_src) __hip_atomic_store(ak_ptr(peer, myslot), (unsigned)inv, RLX);
    }
    {
      const unsigned pub = (unsigned)(uintptr_t)(LAS char*)(smem + T_CNT + 4);
      for (;;) {
        unsigned v;
        asm volatile("ds_read_b32 %0, %1\n\ts_waitcnt lgkmcnt(0)" : "=v"(v) : "v"(pub) : "memory");
        if (__builtin_amdgcn_readfirstlane(v) != 0u) break;
        __builtin_amdgcn_s_sleep(1);
      }
    }
    issue_c(12, 3);
    load_skip();
#pragma unroll
    for (int i = 0; i < 17; ++i) {
      int kt = 15 + i;
      const char* cb = ((kt & 1) ? cb1 : cb0) + (((kt >> 1) + cn) & 15) * 128;
      creg[2 * i] = *(const u32x4*)(cb + co0);
      creg[2 * i + 1] = *(const u32x4*)(cb + co1);
      asm volatile("s_sleep 1" ::: "memory");
    }
    wait_vm0_visible();
    {
      LAS unsigned* c2 = (LAS unsigned*)(smem + T_CNT + 12);
      for (unsigned spins = 0; __hip_atomic_load(c2, __ATOMIC_RELAXED, __HIP_MEMORY_SCOPE_WORKGROUP) < 4u; ++spins) __builtin_amdgcn_s_sleep(1);
      asm volatile("" ::: "memory");
    }
    asm volatile("" ::"v"(tchc));
    if (w == 0) service();
    fin_skip();
  } else {
    const int wm = wave >> 1, wn = wave & 1;
    unsigned p_lre = 0, p_lim = 0, p_ldt = 0;
    if (wave == 3) {
      p_lre = lre[cn * 64 + lane]; p_lim = lim[cn * 64 + lane]; p_ldt = ldt[cn * 64 + lane];
      asm volatile("" ::: "memory");
    }
    const int nj = (wave < cm) ? ((cm - wave + 3) >> 2) : 0;
    unsigned tch0 = 0, tch1 = 0;
    if (wave < 2) {
      const int xp = xcd >> 1, xo = xcd & 1;
      {
        int r = idx * 8 + wave * 4 + (lane >> 4);
        const u16* sx = x + (size_t)(xp * 512 + xo * 256 + r) * DM;
        int br = xp * 256 + r;
        const u16* sb = ((br >> 9) ? bim : bre) + (size_t)(xo * 512 + (br & 511)) * DM;
        tch0 = *(const unsigned*)(sx + (lane & 15) * 64);
        tch1 = *(const unsigned*)(sb + (lane & 15) * 64);
      }
    }
    if (wave == 3) {
      cpx lam, cf;
      disc((u16)p_lre, (u16)p_lim, (u16)p_ldt, lam, cf);
      *(cpx*)(smem + T_LAM + lane * 8) = lam;
      *(cpx*)(smem + T_C2 + lane * 8) = cpx{2.f * cf.r, 2.f * cf.i};
      if (lane < 4) *(unsigned*)(smem + T_CNT + lane * 4) = 0u;
    }
    const int f = (c >> 1) & 7;
    const unsigned a_rd0 = (unsigned)((64 * wm + c) * 128 + ((q ^ f) << 4));
    const unsigned b_rd0 = (unsigned)(16384 + (64 * wn + c) * 128 + ((q ^ f) << 4));
    f32x4 acc[4][4];
#pragma unroll
    for (int i = 0; i < 4; ++i)
#pragma unroll
      for (int j = 0; j < 4; ++j) acc[i][j] = (f32x4){0.f, 0.f, 0.f, 0.f};
    bf16x8 fa0[4], fb0[4], fa1[4], fb1[4];
    auto rd = [&](bf16x8 (&fa)[4], bf16x8 (&fb)[4], int kt, int ks) {
      const char* base = smem + (kt % NST) * STAGE;
      unsigned ao = a_rd0 ^ (ks << 6), bo = b_rd0 ^ (ks << 6);
#pragma unroll
      for (int i = 0; i < 4; ++i) {
        fa[i] = *(const bf16x8*)(base + ao + i * 2048);
        fb[i] = *(const bf16x8*)(base + bo + i * 2048);
      }
    };
    auto mm = [&](bf16x8 (&fa)[4], bf16x8 (&fb)[4]) {
#pragma unroll
      for (int i = 0; i < 4; ++i)
#pragma unroll
        for (int j = 0; j < 4; ++j)
          acc[i][j] = __builtin_amdgcn_mfma_f32_16x16x32_bf16(fa[i], fb[j], acc[i][j], 0, 0, 0);
    };
    bar_lgkm();
    rd(fa0, fb0, 0, 0);
    const int chb = (32 * wn + 2 * c) * 8;
    const int g = 4 * wm + q;
    f32x4 lam4 = *(const f32x4*)(smem + T_LAM + chb);
    f32x4 c24 = *(const f32x4*)(smem + T_C2 + chb);
    cpx l8A, l8B, pA, pB, l16c, l128c, l512c;
    {
      cpx lA = {lam4[0], lam4[1]}, lB = {lam4[2], lam4[3]};
      l8A = csq(csq(csq(lA))); l8B = csq(csq(csq(lB)));
      cpx eA = csq(l8A), eB = csq(l8B);
      pA = cpx{1.f, 0.f}; pB = cpx{1.f, 0.f};
#pragma unroll
      for (int bit = 0; bit < 3; ++bit) {
        cpx mA = cmul(pA, eA), mB = cmul(pB, eB);
        bool on = (g >> bit) & 1;
        pA.r = on ? mA.r : pA.r; pA.i = on ? mA.i : pA.i;
        pB.r = on ? mB.r : pB.r; pB.i = on ? mB.i : pB.i;
        if (bit < 2) { eA = csq(eA); eB = csq(eB); }
      }
      cpx lc = *(const cpx*)(smem + T_LAM + lane * 8);
      l16c = csq(csq(csq(csq(lc))));
      l128c = csq(csq(csq(l16c)));
      l512c = csq(csq(l128c));
      asm volatile("" : "+v"(l8A.r), "+v"(l8A.i), "+v"(l8B.r), "+v"(l8B.i), "+v"(pA.r), "+v"(pA.i), "+v"(pB.r), "+v"(pB.i));
      asm volatile("" : "+v"(l16c.r), "+v"(l16c.i), "+v"(l128c.r), "+v"(l128c.i), "+v"(l512c.r), "+v"(l512c.i), "+v"(lam4), "+v"(c24));
    }
    SB0();
#pragma unroll
    for (int kt = 0; kt < NKT1; ++kt) {
      rd(fa1, fb1, kt, 1);
      mm(fa0, fb0);
#pragma unroll
      for (int i = 0; i < 8; ++i) { SGB(0x100, 1); SGB(0x8, 1); }
      SGB(0x8, 8);
      SB0();
      if (kt < NKT1 - 1) {
        bar_lgkm();
        rd(fa0, fb0, kt + 1, 0);
      }
      mm(fa1, fb1);
      if (kt < NKT1 - 1) {
#pragma unroll
        for (int i = 0; i < 8; ++i) { SGB(0x100, 1); SGB(0x8, 1); }
        SGB(0x8, 8);
      }
      SB0();
    }
    asm volatile("" ::"v"(tch0), "v"(tch1));
    const f32x2 lrA = splat(lam4[0]), liA = splat(lam4[1]), nliA = splat(-lam4[1]);
    const f32x2 lrB = splat(lam4[2]), liB = splat(lam4[3]), nliB = splat(-lam4[3]);
    cpx2 aA = {splat(0.f), splat(0.f)}, aB = {splat(0.f), splat(0.f)};
#pragma unroll
    for (int s = 0; s < 4; ++s) {
      aA = cfma2(lrA, liA, nliA, aA, acc[s][0].xy, acc[s][2].xy);
      aB = cfma2(lrB, liB, nliB, aB, acc[s][1].xy, acc[s][3].xy);
      aA = cfma2(lrA, liA, nliA, aA, acc[s][0].zw, acc[s][2].zw);
      aB = cfma2(lrB, liB, nliB, aB, acc[s][1].zw, acc[s][3].zw);
    }
    const cpx a0A = {aA.r.x, aA.i.x}, a0B = {aB.r.x, aB.i.x};
    {
      cpx sA = cfma(l8A, a0A, cpx{aA.r.y, aA.i.y});
      cpx sB = cfma(l8B, a0B, cpx{aB.r.y, aB.i.y});
      f32x4 t4 = {sA.r, sA.i, sB.r, sB.i};
      *(f32x4*)(smem + T_SEG + g * 512 + chb) = t4;
    }
    bar_lgkm();
    if (wave == 0) {
      cpx sg[8];
#pragma unroll
      for (int gg = 0; gg < 8; ++gg) sg[gg] = *(const cpx*)(smem + T_SEG + (gg * 64 + lane) * 8);
      cpx pre[8];
      cpx a = {0.f, 0.f};
#pragma unroll
      for (int gg = 0; gg < 8; ++gg) { pre[gg] = a; a = cfma(l16c, a, sg[gg]); }
      {
        const unsigned S = *(const unsigned*)(smem + T_PROT + 4);
        u32x4 msg = {__float_as_uint(a.r), S, __float_as_uint(a.i), S};
        if (cm < 15) __builtin_amdgcn_raw_buffer_store_b128(msg, prs, WS_OL + gid * 1024 + lane * 16, 0, 16);
        asm volatile("" ::: "memory");
        if (lane == 0) __hip_atomic_store((LAS unsigned*)(smem + T_CNT + 4), 1u, __ATOMIC_RELAXED, __HIP_MEMORY_SCOPE_WORKGROUP);
      }
#pragma unroll
      for (int gg = 0; gg < 8; ++gg) *(cpx*)(smem + T_PRE + (gg * 64 + lane) * 8) = pre[gg];
    }
    const cpx c2A = {c24[0], c24[1]}, c2B = {c24[2], c24[3]};
#pragma unroll
    for (int s = 0; s < 4; ++s) {
      f32x4 vr = acc[s][0], vi = acc[s][2];
      acc[s][0] = vr * c2A.r - vi * c2A.i;
      acc[s][2] = vi * c2A.r + vr * c2A.i;
      vr = acc[s][1]; vi = acc[s][3];
      acc[s][1] = vr * c2B.r - vi * c2B.i;
      acc[s][3] = vi * c2B.r + vr * c2B.i;
      asm volatile("" : "+v"(acc[s][0]), "+v"(acc[s][1]), "+v"(acc[s][2]), "+v"(acc[s][3]));
    }
    {
      const int w = wave;
      cpx part = {0.f, 0.f};
      if (w < cm) {
        const cpx l128 = l128c, l512 = l512c;
        u32x4 m[4];
        unsigned sj[4];
        {
          LAS unsigned* rdy = (LAS unsigned*)(smem + T_CNT + 8);
          for (unsigned spins = 0; __hip_atomic_load(rdy, __ATOMIC_RELAXED, __HIP_MEMORY_SCOPE_WORKGROUP) == 0u; ++spins) {}
          asm volatile("" ::: "memory");
        }
        unsigned mo[4];
#pragma unroll
        for (int i = 0; i < 4; ++i) {
          const int j = (i < nj) ? (w + 4 * i) : w;
          sj[i] = *(const unsigned*)(smem + T_SIN + j * 4);
          mo[i] = WS_OL + (j * 16 + cn) * 1024 + lane * 16;
        }
        for (unsigned spins = 0;; ++spins) {
          asm volatile("" ::: "memory");
#pragma unroll
          for (int i = 0; i < 4; ++i) m[i] = __builtin_amdgcn_raw_buffer_load_b128(prs, mo[i], 0, 16);
          bool ok = true;
#pragma unroll
          for (int i = 0; i < 4; ++i) ok = ok && m[i][1] == sj[i] && m[i][3] == sj[i];
          if (__all(ok)) break;
          if (spins > (1u << 22)) break;
          if (w == 0 && (spins & 15u) == 15u) service();
        }
        cpx a = {0.f, 0.f};
#pragma unroll
        for (int i = 0; i < 4; ++i) {
          if (i < nj) {
            cpx aj = {__uint_as_float(m[i][0]), __uint_as_float(m[i][2])};
            a = cfma(l512, a, aj);
          }
        }
        int e0 = cm - 1 - (w + 4 * (nj - 1));
        for (int e = 0; e < e0; ++e) a = cmul(a, l128);
        part = a;
      }
      *(cpx*)(smem + T_PART + (w * 64 + lane) * 8) = part;
    }
    {
      wait_lgkm0();
      LAS unsigned* cnt = (LAS unsigned*)(smem + T_CNT);
      if (lane == 0) __hip_atomic_fetch_add(cnt, 1u, __ATOMIC_RELAXED, __HIP_MEMORY_SCOPE_WORKGROUP);
      for (unsigned spins = 0; __hip_atomic_load(cnt, __ATOMIC_RELAXED, __HIP_MEMORY_SCOPE_WORKGROUP) < 4u; ++spins) {
        if (wave == 0 && (spins & 255u) == 255u) service();
      }
      asm volatile("" ::: "memory");
    }
    cpx wA, wB;
    {
      f32x4 t0 = *(const f32x4*)(smem + T_PART + chb), t1 = *(const f32x4*)(smem + T_PART + 512 + chb);
      f32x4 t2 = *(const f32x4*)(smem + T_PART + 1024 + chb), t3 = *(const f32x4*)(smem + T_PART + 1536 + chb);
      f32x4 pr = *(const f32x4*)(smem + T_PRE + g * 512 + chb);
      f32x4 cs = (t0 + t1) + (t2 + t3);
      wA = cfma(pA, cpx{cs[0], cs[1]}, cpx{pr[0], pr[1]});
      wB = cfma(pB, cpx{cs[2], cs[3]}, cpx{pr[2], pr[3]});
    }
    cpx2 uA, uB;
    {
      cpx w1A = cfma(l8A, wA, a0A), w1B = cfma(l8B, wB, a0B);
      cpx u0A = cmul(c2A, wA), u1A = cmul(c2A, w1A), u0B = cmul(c2B, wB), u1B = cmul(c2B, w1B);
      uA.r = (f32x2){u0A.r, u1A.r}; uA.i = (f32x2){u0A.i, u1A.i};
      uB.r = (f32x2){u0B.r, u1B.r}; uB.i = (f32x2){u0B.i, u1B.i};
    }
    const unsigned swz = (unsigned)((c >> 2) | ((q & 1) << 2));
    char* obase = smem + XB + (g * 4 + wn) * 1024 + (c >> 2) * 256 + (c & 3) * 4;
#pragma unroll
    for (int s = 0; s < 4; ++s)
#pragma unroll
      for (int rh = 0; rh < 2; ++rh) {
        if (rh == 0) {
          uA = cfma2(lrA, liA, nliA, uA, acc[s][0].xy, acc[s][2].xy);
          uB = cfma2(lrB, liB, nliB, uB, acc[s][1].xy, acc[s][3].xy);
        } else {
          uA = cfma2(lrA, liA, nliA, uA, acc[s][0].zw, acc[s][2].zw);
          uB = cfma2(lrB, liB, nliB, uB, acc[s][1].zw, acc[s][3].zw);
        }
        unsigned o = (((unsigned)(2 * s + rh)) ^ swz) * 16;
        *(unsigned*)(obase + o) = pack_bf16(uA.r.x, uB.r.x);
        *(unsigned*)(obase + o + 128) = pack_bf16(uA.r.y, uB.r.y);
        *(unsigned*)(obase + o + 2048) = pack_bf16(-uA.i.x, -uB.i.x);
        *(unsigned*)(obase + o + 2048 + 128) = pack_bf16(-uA.i.y, -uB.i.y);
      }
    wait_lgkm0();
#pragma unroll
    for (int qq = 0; qq < 4; ++qq) {
      const unsigned rs = (unsigned)(lane ^ ((lane >> 4) | ((qq & 1) << 2))) * 16;
      const int gq = 4 * wm + qq;
#pragma unroll
      for (int pp = 0; pp < 2; ++pp) {
        u32x4 v = *(const u32x4*)(smem + XB + (gq * 4 + wn + 2 * pp) * 1024 + rs);
        __builtin_amdgcn_raw_buffer_store_b128(v, hrs, (unsigned)((cm * 8 + gq) * 65536 + (cn * 4 + wn + 2 * pp) * 1024 + lane * 16), 0, 16);
      }
    }
    wait_vm<0>();
    wait_vm0_visible();
    {
      wait_lgkm0();
      LAS unsigned* c2 = (LAS unsigned*)(smem + T_CNT + 12);
      if (lane == 0) __hip_atomic_fetch_add(c2, 1u, __ATOMIC_RELAXED, __HIP_MEMORY_SCOPE_WORKGROUP);
      while (__hip_atomic_load(c2, __ATOMIC_RELAXED, __HIP_MEMORY_SCOPE_WORKGROUP) < 4u) {}
      asm volatile("" ::: "memory");
      if (wave == 0) {
        if (lane == 0) __hip_atomic_store(rq_ptr(gid, 1), *(const unsigned*)(smem + T_PROT + 4), RLX);
        LAS unsigned* rdy = (LAS unsigned*)(smem + T_CNT + 8);
        for (unsigned spins = 0; __hip_atomic_load(rdy, __ATOMIC_RELAXED, __HIP_MEMORY_SCOPE_WORKGROUP) == 0u; ++spins) {}
      }
      asm volatile("" ::: "memory");
    }
    if (wave == 0) {
      const unsigned sh = *(const unsigned*)(smem + T_SIN + 64 + (lane & 15) * 4);
      for (unsigned spins = 0;; ++spins) {
        unsigned v = __hip_atomic_load(rq_ptr(cm * 16 + (lane & 15), 1), RLX);
        if (__all((lane & 15) == cn || v == sh)) break;
        if (spins > (1u << 22)) break;
        if ((spins & 15u) == 15u) service();
      }
    }
#pragma unroll
    for (int j = 0; j < 2; ++j)
#pragma unroll
      for (int s = 0; s < 4; ++s) acc2[j][s] = (f32x4){0.f, 0.f, 0.f, 0.f};
  }
  u32x4 hown[2][2];
#pragma unroll
  for (int j = 0; j < 2; ++j) {
    const unsigned rs = (unsigned)(lane ^ ((lane >> 4) | (j << 2))) * 16;
#pragma unroll
    for (int e = 0; e < 2; ++e) hown[e][j] = *(const u32x4*)(smem + XB + ((2 * tq + j) * 4 + 2 * kh + e) * 1024 + rs);
  }
  wait_vm0_visible();
  bar_lgkm();

  {
    const unsigned hwave = (unsigned)((cm * 8 + 2 * tq) * 65536 + kh * 2048 + lane * 16);
    u32x4 hr[HR][2];
    const unsigned c_rd0 = (unsigned)(c * 128 + ((q ^ ((c >> 1) & 7)) << 4));
    bf16x8 cf[2][4];
    unsigned cbase[3][2], cwrap[2];
#pragma unroll
    for (int r = 0; r < 3; ++r)
#pragma unroll
      for (int p = 0; p < 2; ++p) {
        cbase[r][p] = (c_rd0 ^ (p << 6)) + r * 65536 + kh * 8192;
        asm volatile("" : "+v"(cbase[r][p]));
      }
#pragma unroll
    for (int p = 0; p < 2; ++p) {
      cwrap[p] = (c_rd0 ^ (p << 6)) + (kh ? 0 : 18 * 8192);
      asm volatile("" : "+v"(cwrap[p]));
    }
    auto rdc = [&](bf16x8 (&dst)[4], int n) {
      const int ke = 2 * (n >> 1);
      const char* o;
      int off;
      if (ke == 18) { o = smem + cwrap[n & 1]; off = 0; }
      else {
        const int slot = ke < 18 ? ke : ke - 19;
        o = smem + cbase[slot >> 3][n & 1]; off = (slot & 7) * 8192;
      }
#pragma unroll
      for (int s = 0; s < 4; ++s) dst[s] = *(const bf16x8*)(o + off + s * 2048);
    };
    auto ldh = [&](int n, int j) -> u32x4 {
      return __builtin_amdgcn_raw_buffer_load_b128(hrs, hwave + j * 65536 + (n & 1) * 1024, (((n >> 1) + cn) & 15) * 4096, 16);
    };
    unsigned akv = 0;
    if (wave >= 4) {
#pragma unroll
      for (int i = 0; i < 4; ++i) {
        *(u32x4*)(smem + (15 + i) * 8192 + (2 * (wave - 4)) * 1024 + lane * 16) = creg[2 * i];
        *(u32x4*)(smem + (15 + i) * 8192 + (2 * (wave - 4) + 1) * 1024 + lane * 16) = creg[2 * i + 1];
      }
    }
    SB0();
    rdc(cf[0], 0);
    asm volatile("" ::"v"(cf[0][0]), "v"(cf[0][1]), "v"(cf[0][2]), "v"(cf[0][3]) : "memory");
#pragma unroll
    for (int i = 2; i < HR + 2; ++i) {
      hr[i % HR][0] = ldh(i, 0);
      hr[i % HR][1] = ldh(i, 1);
      SB0();
    }
#pragma unroll
    for (int n = 0; n < 32; ++n) {
      if (n == 6 || n == 14) {
        bar_lgkm();
        if (wave >= 4) {
#pragma unroll
          for (int i = (n == 6 ? 0 : 6); i < (n == 6 ? 6 : 13); ++i) {
            *(u32x4*)(smem + i * 8192 + (2 * (wave - 4)) * 1024 + lane * 16) = creg[8 + 2 * i];
            *(u32x4*)(smem + i * 8192 + (2 * (wave - 4) + 1) * 1024 + lane * 16) = creg[8 + 2 * i + 1];
          }
        }
        SB0();
      }
      if (n == 23) { bar_lgkm(); SB0(); }
      if (n == 26) { akv = __hip_atomic_load(ak_ptr(gid, l5), RLX); SB0(); }
      if (n < 31) rdc(cf[(n + 1) & 1], n + 1);
      bf16x8 h0 = __builtin_bit_cast(bf16x8, n < 2 ? hown[n & 1][0] : hr[n % HR][0]);
      bf16x8 h1 = __builtin_bit_cast(bf16x8, n < 2 ? hown[n & 1][1] : hr[n % HR][1]);
#pragma unroll
      for (int s = 0; s < 4; ++s) {
        acc2[0][s] = __builtin_amdgcn_mfma_f32_16x16x32_bf16(cf[n & 1][s], h0, acc2[0][s], 0, 0, 0);
        acc2[1][s] = __builtin_amdgcn_mfma_f32_16x16x32_bf16(cf[n & 1][s], h1, acc2[1][s], 0, 0, 0);
      }
      if (n >= 2 && n < 32 - HR) { hr[n % HR][0] = ldh(n + HR, 0); hr[n % HR][1] = ldh(n + HR, 1); }
      if (n < 31) { SGB(0x100, 4); }
      SGB(0x8, 8);
      if (n >= 2 && n < 32 - HR) SGB(0x20, 2);
      SB0();
    }
    char* xw = smem + 13 * 8192 + wave * 4096 + lane * 16;
    char* xr = smem + 13 * 8192 + (wave ^ 4) * 4096 + lane * 16;
#pragma unroll
    for (int s = 0; s < 4; ++s) *(f32x4*)(xw + s * 1024) = kh ? acc2[0][s] : acc2[1][s];
    bar_lgkm();
    const int t = cm * 128 + 32 * tq + 16 * kh + c;
#pragma unroll
    for (int s = 0; s < 4; ++s) {
      f32x4 o = *(const f32x4*)(xr + s * 1024);
      f32x4 mine = kh ? acc2[1][s] : acc2[0][s];
      __builtin_nontemporal_store(mine + o, (f32x4*)(y + (size_t)t * DM + cn * 64 + 16 * s + 4 * q));
    }
    if (wave == 0) {
      const unsigned S = *(const unsigned*)(smem + T_PROT + 4);
      for (unsigned spins = 0;; ++spins) {
        if (__all(!is_dst || akv == S)) break;
        if (spins > (1u << 22)) break;
        service();
        akv = __hip_atomic_load(ak_ptr(gid, l5), RLX);
      }
    }
  }
}

extern "C" void kernel_launch(void* const* d_in, const int* in_sizes, int n_in, void* d_out, int out_size,
                              void* d_ws, size_t ws_size, hipStream_t stream) {
  const u16* x = (const u16*)d_in[0];
  const u16* lre = (const u16*)d_in[1];
  const u16* lim = (const u16*)d_in[2];
  const u16* bre = (const u16*)d_in[3];
  const u16* bim = (const u16*)d_in[4];
  const u16* cre = (const u16*)d_in[5];
  const u16* cim = (const u16*)d_in[6];
  const u16* ldt = (const u16*)d_in[7];
  const u16* dsk = (const u16*)d_in[8];
  float* y = (float*)d_out;
  u64* gran = (u64*)d_ws;
  u16* hbuf = (u16*)((char*)d_ws + (1u << 20));
  s5_fused<<<256, 512, 0, stream>>>(x, lre, lim, bre, bim, cre, cim, ldt, dsk, y, hbuf, gran);
}
```
